```python
import torch

def _softmax(x, axis=-1):
    return torch.softmax(x, dim=axis)
def _gelu(x):
    return torch.nn.functional.gelu(x, approximate="tanh")
def _silu(x):
    return torch.nn.functional.silu(x)
def _sigmoid(x):
    return torch.sigmoid(x)
def _layernorm(x, axis=-1, eps=1e-5):
    m = x.mean(dim=axis, keepdim=True)
    v = x.var(dim=axis, keepdim=True, unbiased=False)
    return (x - m) / torch.sqrt(v + eps)
def _scatter_add(tgt, idx, src, axis=0):
    return tgt.clone().index_add_(axis, idx, src)
def _segment_sum(src, seg, n):
    out = torch.zeros((n,) + src.shape[1:], dtype=src.dtype)
    return out.index_add_(0, seg, src)
def _pad(x, pw):
    flat = tuple(v for pair in reversed(pw) for v in pair)
    return torch.nn.functional.pad(x, flat)

kernel_name = 'parallel_sum__gqa_block_cached__glu_gated_swiglu__decode'

def setup_inputs():

    _w = (lambda a: (lambda t: t.to(torch.bfloat16) if t.is_floating_point() else t)(torch.as_tensor(a)))
    def _A_setup():
        B, Sq, D = 1, 1, 640
        Hq, Hkv, Sk, Dh = 32, 32, 32768, 128
        assert Hq % Hkv == 0, (Hq, Hkv)
        return {
            "x": _w((lambda *s: torch.randn(*s, dtype=torch.bfloat16) * 0.05)(B, Sq, D)),
            "k_cache": _w((lambda *s: torch.randn(*s, dtype=torch.bfloat16) * 3.2)(B, Hkv, Sk, Dh)),
            "v_cache": _w((lambda *s: torch.randn(*s, dtype=torch.bfloat16) * 1.6)(B, Hkv, Sk, Dh)),
            "wq": _w((lambda *s: torch.randn(*s, dtype=torch.bfloat16) * 3.2)(D, Hq * Dh) * (D ** -0.5)),
            "wo": _w((lambda *s: torch.randn(*s, dtype=torch.bfloat16) * 1.6)(Hq * Dh, D) * ((Hq * Dh) ** -0.5)),
        }
    _A_in = _A_setup()

    def _B_setup():
        B, S, D, F = 1, 1, 640, 1728
        return {
            "x": _w((lambda *s: torch.randn(*s, dtype=torch.bfloat16) * 0.05)(B, S, D)),
            "w1": _w((lambda *s: torch.randn(*s, dtype=torch.bfloat16) * 1.6)(D, F) * (2.0 / D) ** 0.5),
            "w3": _w((lambda *s: torch.randn(*s, dtype=torch.bfloat16) * 1.6)(D, F) * (2.0 / D) ** 0.5),
            "w2": _w((lambda *s: torch.randn(*s, dtype=torch.bfloat16) * 1.6)(F, D) * (2.0 / F) ** 0.5),
        }
    _B_in = _B_setup()

    return {
        "A_x": _A_in["x"],
        "A_k_cache": _A_in["k_cache"],
        "A_v_cache": _A_in["v_cache"],
        "A_wq": _A_in["wq"],
        "A_wo": _A_in["wo"],
        "B_x": _B_in["x"],
        "B_w1": _B_in["w1"],
        "B_w3": _B_in["w3"],
        "B_w2": _B_in["w2"],
    }

def reference(A_x, A_k_cache, A_v_cache, A_wq, A_wo, B_x, B_w1, B_w3, B_w2):
    def _A_ref(x, k_cache, v_cache, wq, wo):
        B, Sq, D = x.shape
        Hq, Dh = 32, 128
        Hkv = k_cache.shape[1]
        rep = Hq // Hkv
        q = (lambda x, *d: x.permute(*d))((x @ wq).reshape(B, Sq, Hq, Dh), 0, 2, 1, 3)
        k = (lambda x, n, axis: torch.repeat_interleave(x, n, dim=axis))(k_cache, rep, axis=1)
        v = (lambda x, n, axis: torch.repeat_interleave(x, n, dim=axis))(v_cache, rep, axis=1)
        scores = torch.einsum("bhqd,bhkd->bhqk", q, k) * (Dh ** -0.5)
        p = _softmax(scores, axis=-1)
        o = torch.einsum("bhqk,bhkd->bhqd", p, v)
        return (lambda x, *d: x.permute(*d))(o, 0, 2, 1, 3).reshape(B, Sq, Hq * Dh) @ wo

    def _B_ref(x, w1, w3, w2):
        gate = torch.einsum("bsd,df->bsf", x, w1)
        up = torch.einsum("bsd,df->bsf", x, w3)
        h = _silu(gate) * up
        out = torch.einsum("bsf,fd->bsd", h, w2)
        return out

    x = A_x
    xn = _layernorm(x, axis=-1)
    return x + _A_ref(xn, A_k_cache, A_v_cache, A_wq, A_wo) + _B_ref(xn, B_w1, B_w3, B_w2)
```

```cpp
#include <hip/hip_runtime.h>
#include <hip/hip_bf16.h>
#include <stdint.h>

#define MIMIC 1

typedef unsigned short u16;
using u32x4 = __attribute__((ext_vector_type(4))) unsigned;
using bf16x2 = __attribute__((ext_vector_type(2))) __bf16;

constexpr int D = 640, H = 32, SK = 32768, DH = 128, F = 1728;
constexpr int NSPLIT = 16;

__device__ __forceinline__ float bf2f(u16 v) { return __uint_as_float(((unsigned)v) << 16); }
__device__ __forceinline__ u16 f2bf(float f) { __bf16 b = (__bf16)f; return __builtin_bit_cast(u16, b); }
__device__ __forceinline__ float rbf(float f) {
  return bf2f(f2bf(f));
}
__device__ __forceinline__ float dot2(unsigned k, unsigned q, float acc) {
  return __builtin_amdgcn_fdot2_f32_bf16(__builtin_bit_cast(bf16x2, k), __builtin_bit_cast(bf16x2, q), acc, false);
}

__device__ void layernorm(const u16* x, float* xn  , float* red) {
  int t = threadIdx.x;
  float s = 0.f;
  for (int i = t; i < D; i += 256) s += bf2f(x[i]);
  for (int o = 32; o > 0; o >>= 1) s += __shfl_xor(s, o);
  if ((t & 63) == 0) red[t >> 6] = s;
  __syncthreads();
  float mean = (red[0] + red[1] + red[2] + red[3]) / (float)D;
  __syncthreads();
  float v = 0.f;
  for (int i = t; i < D; i += 256) { float d = bf2f(x[i]) - mean; v += d * d; }
  for (int o = 32; o > 0; o >>= 1) v += __shfl_xor(v, o);
  if ((t & 63) == 0) red[t >> 6] = v;
  __syncthreads();
  float var = (red[0] + red[1] + red[2] + red[3]) / (float)D;
  float mb = rbf(mean), vb = rbf(var);
  float den = rbf(sqrtf(rbf(vb + 1e-5f)));
  for (int i = t; i < D; i += 256) xn[i] = rbf(rbf(bf2f(x[i]) - mb) / den);
  __syncthreads();
}

__global__ void __launch_bounds__(256) k_pro(const u16* x, const u16* wq, const u16* w1, const u16* w3, float* q, float* hid) {
  __shared__ float xn[D];
  __shared__ float red[4];
  layernorm(x, xn, red);
  int b = blockIdx.x, t = threadIdx.x;
  if (b < 16) {
    int c = b * 256 + t;
    float a = 0.f;
    for (int i = 0; i < D; ++i) a = fmaf(xn[i], bf2f(wq[(size_t)i * 4096 + c]), a);
    q[c] = bf2f(f2bf(a));
  } else {
    int c = (b - 16) * 256 + t;
    if (c < F) {
      float g = 0.f, u = 0.f;
      for (int i = 0; i < D; ++i) { g = fmaf(xn[i], bf2f(w1[(size_t)i * F + c]), g); u = fmaf(xn[i], bf2f(w3[(size_t)i * F + c]), u); }
      g = rbf(g); u = rbf(u);
      float s = rbf(g / (1.f + __expf(-g)));
      hid[c] = rbf(s * u);
    }
  }
}

__global__ void __launch_bounds__(256) k_attn(const u16* K, const u16* V, const float* q, float* part) {
  __shared__ float sm[4][132];
  int s = blockIdx.x, h = blockIdx.y;
  int t = threadIdx.x, lane = t & 63, w = t >> 6, r = lane >> 4, c = lane & 15;
  unsigned qp[4];
  for (int k = 0; k < 4; ++k) qp[k] = (unsigned)f2bf(q[h * DH + 8 * c + 2 * k]) | ((unsigned)f2bf(q[h * DH + 8 * c + 2 * k + 1]) << 16);
  const int per = SK / NSPLIT;
  const u32x4* Kp = (const u32x4*)(K + ((size_t)h * SK + s * per) * DH);
  const u32x4* Vp = (const u32x4*)(V + ((size_t)h * SK + s * per) * DH);
  float m = -INFINITY, l = 0.f, acc[8] = {0, 0, 0, 0, 0, 0, 0, 0};
  for (int i = w; i < per / 16; i += 4) {
    u32x4 kk[4], vv[4];
#pragma unroll
    for (int j = 0; j < 4; ++j) { kk[j] = __builtin_nontemporal_load(Kp + i * 256 + j * 64 + lane); vv[j] = __builtin_nontemporal_load(Vp + i * 256 + j * 64 + lane); }
    float sc[4];
#pragma unroll
    for (int j = 0; j < 4; ++j) {
      float d = 0.f;
#pragma unroll
      for (int k = 0; k < 4; ++k) d = dot2(kk[j][k], qp[k], d);
      d += __shfl_xor(d, 1); d += __shfl_xor(d, 2); d += __shfl_xor(d, 4); d += __shfl_xor(d, 8);
      sc[j] = rbf(rbf(d) * 0.08838834764831845f);
    }
    float mx = fmaxf(fmaxf(sc[0], sc[1]), fmaxf(sc[2], sc[3]));
    float mn = fmaxf(m, mx);
    float al = __expf(m - mn);
    m = mn; l *= al;
#pragma unroll
    for (int k = 0; k < 8; ++k) acc[k] *= al;
#pragma unroll
    for (int j = 0; j < 4; ++j) {
      float p = __expf(sc[j] - mn);
      l += p;
#pragma unroll
      for (int k = 0; k < 4; ++k) {
        acc[2 * k] = fmaf(p, __uint_as_float(vv[j][k] << 16), acc[2 * k]);
        acc[2 * k + 1] = fmaf(p, __uint_as_float(vv[j][k] & 0xffff0000u), acc[2 * k + 1]);
      }
    }
  }
  float M = fmaxf(m, __shfl_xor(m, 16)); M = fmaxf(M, __shfl_xor(M, 32));
  float f = __expf(m - M);
  l *= f; l += __shfl_xor(l, 16); l += __shfl_xor(l, 32);
#pragma unroll
  for (int k = 0; k < 8; ++k) { float a = acc[k] * f; a += __shfl_xor(a, 16); a += __shfl_xor(a, 32); acc[k] = a; }
  if (r == 0) { for (int k = 0; k < 8; ++k) sm[w][4 + 8 * c + k] = acc[k]; if (c == 0) { sm[w][0] = M; sm[w][1] = l; } }
  __syncthreads();
  if (t < 128) {
    float MM = fmaxf(fmaxf(sm[0][0], sm[1][0]), fmaxf(sm[2][0], sm[3][0]));
    float L = 0.f, o = 0.f;
    for (int ww = 0; ww < 4; ++ww) { float e = __expf(sm[ww][0] - MM); L += e * sm[ww][1]; o += e * sm[ww][4 + t]; }
    float* pp = part + (size_t)(h * NSPLIT + s) * 132;
    pp[4 + t] = o;
    if (t == 0) { pp[0] = MM; pp[1] = L; }
  }
}

__global__ void __launch_bounds__(256) k_fin(const u16* x, const u16* wo, const u16* w2, const float* part, const float* hid, u16* out) {
  __shared__ float o[4096];
  __shared__ float red[2][4][64];
  int t = threadIdx.x;
  for (int i = t; i < 4096; i += 256) {
    int h = i >> 7, d = i & 127;
    float M = -INFINITY;
    for (int s = 0; s < NSPLIT; ++s) M = fmaxf(M, part[(h * NSPLIT + s) * 132]);
    float L = 0.f, a = 0.f;
    for (int s = 0; s < NSPLIT; ++s) { const float* pp = part + (h * NSPLIT + s) * 132; float e = __expf(pp[0] - M); L += e * pp[1]; a += e * pp[4 + d]; }
    o[i] = rbf(a / L);
  }
  __syncthreads();
  int c = blockIdx.x * 64 + (t & 63), g = t >> 6;
  float a = 0.f, b = 0.f;
  for (int i = g; i < 4096; i += 4) a = fmaf(o[i], bf2f(wo[(size_t)i * D + c]), a);
  for (int i = g; i < F; i += 4) b = fmaf(hid[i], bf2f(w2[(size_t)i * D + c]), b);
  red[0][g][t & 63] = a; red[1][g][t & 63] = b;
  __syncthreads();
  if (t < 64) {
    float A = rbf(red[0][0][t] + red[0][1][t] + red[0][2][t] + red[0][3][t]);
    float B = rbf(red[1][0][t] + red[1][1][t] + red[1][2][t] + red[1][3][t]);
    out[c] = f2bf(rbf(bf2f(x[c]) + A) + B);
  }
}

extern "C" void kernel_launch(void* const* d_in, const int* in_sizes, int n_in, void* d_out, int out_size, void* d_ws, size_t ws_size, hipStream_t stream) {
  const u16* x = (const u16*)d_in[0];
  const u16* K = (const u16*)d_in[1];
  const u16* V = (const u16*)d_in[2];
  const u16* wq = (const u16*)d_in[3];
  const u16* wo = (const u16*)d_in[4];
  const u16* w1 = (const u16*)d_in[6];
  const u16* w3 = (const u16*)d_in[7];
  const u16* w2 = (const u16*)d_in[8];
  float* q = (float*)d_ws;
  float* hid = q + 4096;
  float* part = hid + 2048;
  k_pro<<<23, 256, 0, stream>>>(x, wq, w1, w3, q, hid);
  k_attn<<<dim3(NSPLIT, H), 256, 0, stream>>>(K, V, q, part);
  k_fin<<<10, 256, 0, stream>>>(x, wo, w2, part, hid, (u16*)d_out);
}
```

```cpp
#include <hip/hip_runtime.h>
#include <hip/hip_bf16.h>
#include <stdint.h>

#define MIMIC 1

typedef unsigned short u16;
using u32x4 = __attribute__((ext_vector_type(4))) unsigned;
using bf16x2 = __attribute__((ext_vector_type(2))) __bf16;

constexpr int D = 640, H = 32, SK = 32768, DH = 128, F = 1728;
constexpr int NSPLIT = 16;

__device__ __forceinline__ float bf2f(u16 v) { return __uint_as_float(((unsigned)v) << 16); }
__device__ __forceinline__ u16 f2bf(float f) { __bf16 b = (__bf16)f; return __builtin_bit_cast(u16, b); }
__device__ __forceinline__ float rbf(float f) {
  return bf2f(f2bf(f));
}
__device__ __forceinline__ float dot2(unsigned k, unsigned q, float acc) {
  return __builtin_amdgcn_fdot2_f32_bf16(__builtin_bit_cast(bf16x2, k), __builtin_bit_cast(bf16x2, q), acc, false);
}

__device__ void layernorm(const u16* x, float* xn  , float* red) {
  int t = threadIdx.x;
  float s = 0.f;
  for (int i = t; i < D; i += 256) s += bf2f(x[i]);
  for (int o = 32; o > 0; o >>= 1) s += __shfl_xor(s, o);
  if ((t & 63) == 0) red[t >> 6] = s;
  __syncthreads();
  float mean = (red[0] + red[1] + red[2] + red[3]) / (float)D;
  __syncthreads();
  float v = 0.f;
  for (int i = t; i < D; i += 256) { float d = bf2f(x[i]) - mean; v += d * d; }
  for (int o = 32; o > 0; o >>= 1) v += __shfl_xor(v, o);
  if ((t & 63) == 0) red[t >> 6] = v;
  __syncthreads();
  float var = (red[0] + red[1] + red[2] + red[3]) / (float)D;
  float mb = rbf(mean), vb = rbf(var);
  float den = rbf(sqrtf(rbf(vb + 1e-5f)));
  for (int i = t; i < D; i += 256) xn[i] = rbf(rbf(bf2f(x[i]) - mb) / den);
  __syncthreads();
}

__global__ void __launch_bounds__(256) k_pro(const u16* x, const u16* wq, const u16* w1, const u16* w3, float* q, float* hid) {
  __shared__ float xn[D];
  __shared__ float red[4];
  layernorm(x, xn, red);
  int b = blockIdx.x, t = threadIdx.x;
  if (b < 16) {
    int c = b * 256 + t;
    float a = 0.f;
    for (int i = 0; i < D; ++i) a = fmaf(xn[i], bf2f(wq[(size_t)i * 4096 + c]), a);
    q[c] = bf2f(f2bf(a));
  } else {
    int c = (b - 16) * 256 + t;
    if (c < F) {
      float g = 0.f, u = 0.f;
      for (int i = 0; i < D; ++i) { g = fmaf(xn[i], bf2f(w1[(size_t)i * F + c]), g); u = fmaf(xn[i], bf2f(w3[(size_t)i * F + c]), u); }
      g = rbf(g); u = rbf(u);
      float s = rbf(g / (1.f + __expf(-g)));
      hid[c] = rbf(s * u);
    }
  }
}

__global__ void __launch_bounds__(256) k_attn(const u16* K, const u16* V, const float* q, float* part) {
  __shared__ float sm[4][132];
  int s = blockIdx.x, h = blockIdx.y;
  int t = threadIdx.x, lane = t & 63, w = t >> 6, r = lane >> 4, c = lane & 15;
  unsigned qp[4];
  for (int k = 0; k < 4; ++k) qp[k] = (unsigned)f2bf(q[h * DH + 8 * c + 2 * k]) | ((unsigned)f2bf(q[h * DH + 8 * c + 2 * k + 1]) << 16);
  const int per = SK / NSPLIT;
  const u32x4* Kp = (const u32x4*)(K + ((size_t)h * SK + s * per) * DH);
  const u32x4* Vp = (const u32x4*)(V + ((size_t)h * SK + s * per) * DH);
  float m = -INFINITY, l = 0.f, acc[8] = {0, 0, 0, 0, 0, 0, 0, 0};
  for (int i = w; i < per / 16; i += 4) {
    u32x4 kk[4], vv[4];
#pragma unroll
    for (int j = 0; j < 4; ++j) { kk[j] = __builtin_nontemporal_load(Kp + i * 256 + j * 64 + lane); vv[j] = __builtin_nontemporal_load(Vp + i * 256 + j * 64 + lane); }
    float sc[4];
#pragma unroll
    for (int j = 0; j < 4; ++j) {
      float d = 0.f;
#pragma unroll
      for (int k = 0; k < 4; ++k) d = dot2(kk[j][k], qp[k], d);
      d += __shfl_xor(d, 1); d += __shfl_xor(d, 2); d += __shfl_xor(d, 4); d += __shfl_xor(d, 8);
      sc[j] = rbf(rbf(d) * 0.08838834764831845f);
    }
    float mx = fmaxf(fmaxf(sc[0], sc[1]), fmaxf(sc[2], sc[3]));
    float mn = fmaxf(m, mx);
    float al = __expf(m - mn);
    m = mn; l *= al;
#pragma unroll
    for (int k = 0; k < 8; ++k) acc[k] *= al;
#pragma unroll
    for (int j = 0; j < 4; ++j) {
      float p = __expf(sc[j] - mn);
      l += p;
#pragma unroll
      for (int k = 0; k < 4; ++k) {
        acc[2 * k] = fmaf(p, __uint_as_float(vv[j][k] << 16), acc[2 * k]);
        acc[2 * k + 1] = fmaf(p, __uint_as_float(vv[j][k] & 0xffff0000u), acc[2 * k + 1]);
      }
    }
  }
  float M = fmaxf(m, __shfl_xor(m, 16)); M = fmaxf(M, __shfl_xor(M, 32));
  float f = __expf(m - M);
  l *= f; l += __shfl_xor(l, 16); l += __shfl_xor(l, 32);
#pragma unroll
  for (int k = 0; k < 8; ++k) { float a = acc[k] * f; a += __shfl_xor(a, 16); a += __shfl_xor(a, 32); acc[k] = a; }
  if (r == 0) { for (int k = 0; k < 8; ++k) sm[w][4 + 8 * c + k] = acc[k]; if (c == 0) { sm[w][0] = M; sm[w][1] = l; } }
  __syncthreads();
  if (t < 128) {
    float MM = fmaxf(fmaxf(sm[0][0], sm[1][0]), fmaxf(sm[2][0], sm[3][0]));
    float L = 0.f, o = 0.f;
    for (int ww = 0; ww < 4; ++ww) { float e = __expf(sm[ww][0] - MM); L += e * sm[ww][1]; o += e * sm[ww][4 + t]; }
    float* pp = part + (size_t)(h * NSPLIT + s) * 132;
    pp[4 + t] = o;
    if (t == 0) { pp[0] = MM; pp[1] = L; }
  }
}

__global__ void __launch_bounds__(256) k_fin(const u16* x, const u16* wo, const u16* w2, const float* part, const float* hid, u16* out) {
  __shared__ float o[4096];
  __shared__ float red[2][4][64];
  int t = threadIdx.x;
  for (int i = t; i < 4096; i += 256) {
    int h = i >> 7, d = i & 127;
    float M = -INFINITY;
    for (int s = 0; s < NSPLIT; ++s) M = fmaxf(M, part[(h * NSPLIT + s) * 132]);
    float L = 0.f, a = 0.f;
    for (int s = 0; s < NSPLIT; ++s) { const float* pp = part + (h * NSPLIT + s) * 132; float e = __expf(pp[0] - M); L += e * pp[1]; a += e * pp[4 + d]; }
    o[i] = rbf(a / L);
  }
  __syncthreads();
  int c = blockIdx.x * 64 + (t & 63), g = t >> 6;
  float a = 0.f, b = 0.f;
  for (int i = g; i < 4096; i += 4) a = fmaf(o[i], bf2f(wo[(size_t)i * D + c]), a);
  for (int i = g; i < F; i += 4) b = fmaf(hid[i], bf2f(w2[(size_t)i * D + c]), b);
  red[0][g][t & 63] = a; red[1][g][t & 63] = b;
  __syncthreads();
  if (t < 64) {
    float A = rbf(red[0][0][t] + red[0][1][t] + red[0][2][t] + red[0][3][t]);
    float B = rbf(red[1][0][t] + red[1][1][t] + red[1][2][t] + red[1][3][t]);
    out[c] = f2bf(rbf(bf2f(x[c]) + A) + B);
  }
}

#include <stdio.h>
__global__ void k_probe(const char* K, const char* V, const char* wq, const char* ws, unsigned* out) {
  unsigned xcc; asm volatile("s_getreg_b32 %0, hwreg(HW_REG_XCC_ID)" : "=s"(xcc));
  if (threadIdx.x != 0) return;
  unsigned acc = 0;
  printf("xcc %u K %p V %p wq %p ws %p\n", xcc, K, V, wq, ws);
  for (int tn = 0; tn < 2; ++tn) {
    const char* base = tn ? V : K;
    for (int pg = 0; pg < 128; ++pg) {
      unsigned lat[8];
#pragma unroll
      for (int b = 0; b < 8; ++b) {
        unsigned best = 0;
        for (int r = 0; r < 3; ++r) {
          const char* p = base + (size_t)pg * 2097152 + b * 4096 + r * 256 + 1024;
          unsigned long long t0, t1; unsigned v, v0;
          asm volatile("global_load_dword %0, %1, off nt\n\ts_waitcnt vmcnt(0)" : "=v"(v0) : "v"(p) : "memory");
          asm volatile("s_memtime %0\n\ts_waitcnt lgkmcnt(0)" : "=s"(t0) :: "memory");
          asm volatile("global_load_dword %0, %1, off offset:128 nt\n\ts_waitcnt vmcnt(0)" : "=v"(v) : "v"(p) : "memory");
          asm volatile("s_memtime %0\n\ts_waitcnt lgkmcnt(0)" : "=s"(t1) :: "memory");
          acc ^= v ^ v0;
          best += (unsigned)(t1 - t0);
        }
        lat[b] = best / 3;
      }
      out[(tn * 128 + pg) * 8 + 0] = lat[0]; out[(tn * 128 + pg) * 8 + 1] = lat[1]; out[(tn * 128 + pg) * 8 + 2] = lat[2]; out[(tn * 128 + pg) * 8 + 3] = lat[3];
      out[(tn * 128 + pg) * 8 + 4] = lat[4]; out[(tn * 128 + pg) * 8 + 5] = lat[5]; out[(tn * 128 + pg) * 8 + 6] = lat[6]; out[(tn * 128 + pg) * 8 + 7] = lat[7];
    }
  }
  for (int i = 0; i < 256; ++i) {
    unsigned* o = out + i * 8;
    int best = 0; unsigned bv = ~0u;
    for (int b = 0; b < 8; b += 2) { unsigned s = o[b] + o[b + 1]; if (s < bv) { bv = s; best = b; } }
    if (i % 16 == 0) printf("\n%s pg %3d [%u %u %u %u %u %u %u %u]: ", i < 128 ? "K" : "V", i & 127, o[0], o[1], o[2], o[3], o[4], o[5], o[6], o[7]);
    printf("%d%c ", best, o[best] < o[best + 1] ? 'a' : 'b');
  }
  printf("\n");
  if (acc == 0x12345) out[4096] = acc;
}

extern "C" void kernel_launch(void* const* d_in, const int* in_sizes, int n_in, void* d_out, int out_size, void* d_ws, size_t ws_size, hipStream_t stream) {
  const u16* x = (const u16*)d_in[0];
  const u16* K = (const u16*)d_in[1];
  const u16* V = (const u16*)d_in[2];
  const u16* wq = (const u16*)d_in[3];
  const u16* wo = (const u16*)d_in[4];
  const u16* w1 = (const u16*)d_in[6];
  const u16* w3 = (const u16*)d_in[7];
  const u16* w2 = (const u16*)d_in[8];
  float* q = (float*)d_ws;
  float* hid = q + 4096;
  float* part = hid + 2048;
  k_probe<<<1, 64, 0, stream>>>((const char*)K, (const char*)V, (const char*)wq, (const char*)d_ws, (unsigned*)d_ws + (1 << 20));
  k_pro<<<23, 256, 0, stream>>>(x, wq, w1, w3, q, hid);
  k_attn<<<dim3(NSPLIT, H), 256, 0, stream>>>(K, V, q, part);
  k_fin<<<10, 256, 0, stream>>>(x, wo, w2, part, hid, (u16*)d_out);
}
```

```cpp
#include <hip/hip_runtime.h>
#include <hip/hip_bf16.h>
#include <stdint.h>

constexpr int NS = 3;

typedef unsigned short u16;
typedef unsigned long long u64;
using u32x4 = __attribute__((ext_vector_type(4))) unsigned;
typedef u32x4 u32x4_a4 __attribute__((aligned(4)));
using f32x2 = __attribute__((ext_vector_type(2))) float;
using f32x4 = __attribute__((ext_vector_type(4))) float;
using bf16x8 = __attribute__((ext_vector_type(8))) __bf16;
#define RLX_AGENT __ATOMIC_RELAXED, __HIP_MEMORY_SCOPE_AGENT
#define LAS __attribute__((address_space(3)))
#define LDS(T, a) (*(volatile LAS T*)(a))
#pragma clang diagnostic ignored "-Wint-to-pointer-cast"

constexpr int DM = 640, NH = 32, DH = 128, FF = 1728, NG = 27;
constexpr float LOG2E = 1.4426950408889634f, LN2 = 0.6931471805599453f;
constexpr unsigned SPIN_G = 1u << 17, SPIN_L = 1u << 20;
constexpr int CP0 = 12, CP1 = 19, CP2 = 25;
constexpr int GAINB = 40, DEADB = 10;

constexpr size_t WS_EPOCH = 0;
constexpr size_t WS_VOTE = 64;
constexpr size_t WS_XF = 320;
constexpr size_t WS_PG = 576;
constexpr size_t WS_ML = 1536;
constexpr size_t WS_QP = 2048;
constexpr size_t WS_GP = WS_QP + 32 * 8 * 128;
constexpr size_t WS_BP = WS_GP + 32 * 16 * 128;
constexpr size_t WS_PP = WS_BP + 32 * 640;
constexpr size_t WS_AP = WS_PP + 32 * 8 * 128;
constexpr size_t WS_X0 = WS_AP + 32 * 640;
constexpr size_t WS_SX = 1u << 19;

constexpr int L_Q = 0;
constexpr int L_PROG = 256;
constexpr int L_CNT = 320;
constexpr int L_XN = 384, L_OUT = 1664, L_PART = 4096;
constexpr int L_SOWN = 8192, L_SIN = L_SOWN + 16384, L_BYTES = 84 * 1024;

__device__ __forceinline__ float bflo(unsigned v) { return __uint_as_float(v << 16); }
__device__ __forceinline__ float bfhi(unsigned v) { return __uint_as_float(v & 0xffff0000u); }
__device__ __forceinline__ u16 f2bf(float f) { __bf16 b = (__bf16)f; return __builtin_bit_cast(u16, b); }
__device__ __forceinline__ float rbf(float f) { return bflo(f2bf(f)); }
__device__ __forceinline__ unsigned pair_bf(float f) { const unsigned v = f2bf(f); return v | (v << 16); }
template <int CTRL> __device__ __forceinline__ unsigned dppu(unsigned x) { return __builtin_amdgcn_mov_dpp(x, CTRL, 0xf, 0xf, true); }
template <int CTRL> __device__ __forceinline__ float dpp(float x) { return __uint_as_float(dppu<CTRL>(__float_as_uint(x))); }
constexpr int ROR8 = 0x128, ROR4 = 0x124, ROR2 = 0x122, ROR1 = 0x121, BCAST = 0x150;
__device__ __forceinline__ float add32(float a, float b) {
  auto r = __builtin_amdgcn_permlane32_swap(__float_as_uint(a), __float_as_uint(b), false, false);
  return __uint_as_float(r[0]) + __uint_as_float(r[1]);
}
__device__ __forceinline__ float add16(float a, float b) {
  auto r = __builtin_amdgcn_permlane16_swap(__float_as_uint(a), __float_as_uint(b), false, false);
  return __uint_as_float(r[0]) + __uint_as_float(r[1]);
}
__device__ __forceinline__ float max32(float a) {
  auto r = __builtin_amdgcn_permlane32_swap(__float_as_uint(a), __float_as_uint(a), false, false);
  return fmaxf(__uint_as_float(r[0]), __uint_as_float(r[1]));
}
__device__ __forceinline__ float max16(float a) {
  auto r = __builtin_amdgcn_permlane16_swap(__float_as_uint(a), __float_as_uint(a), false, false);
  return fmaxf(__uint_as_float(r[0]), __uint_as_float(r[1]));
}
__device__ __forceinline__ float row_sum(float v) {
  v += dpp<ROR8>(v); v += dpp<ROR4>(v); v += dpp<ROR2>(v);
  return v + dpp<ROR1>(v);
}
__device__ __forceinline__ float wave_sum(float v) {
  v = row_sum(v);
  v = add16(v, v);
  return add32(v, v);
}
__device__ __forceinline__ void put(u64* g, unsigned tag, unsigned v) { __hip_atomic_store(g, ((u64)tag << 32) | v, RLX_AGENT); }
__device__ __forceinline__ void put(u64* g, unsigned tag, float v) { put(g, tag, __float_as_uint(v)); }
template <int N> __device__ __forceinline__ void fetch(const u64* const (&g)[N], u64 (&r)[N]) {
#pragma unroll
  for (int k = 0; k < N; ++k) r[k] = __hip_atomic_load(g[k], RLX_AGENT);
  asm volatile("" ::: "memory");
}
template <int N> __device__ __forceinline__ bool check(const u64 (&r)[N], unsigned tag, float (&v)[N]) {
  bool ok = true;
#pragma unroll
  for (int k = 0; k < N; ++k) { v[k] = __uint_as_float((unsigned)r[k]); ok &= (unsigned)(r[k] >> 32) == tag; }
  return __all(ok);
}
template <int N, int NAP> __device__ __forceinline__ void gather(const u64* const (&g)[N], unsigned tag, float (&v)[N]) {
  for (unsigned it = 0;; ++it) {
    u64 r[N];
    fetch(g, r);
    if (check(r, tag, v) || it >= SPIN_G) return;
    if (NAP) __builtin_amdgcn_s_sleep(NAP);
  }
}
__device__ __forceinline__ void poll4(const u64* p, unsigned tag, float (&v)[4]) {
  unsigned t, u, c;
  asm volatile(
      "global_load_dwordx2 v[200:201], %[p], off offset:0 sc1\n"
      "global_load_dwordx2 v[202:203], %[p], off offset:1024 sc1\n"
      "global_load_dwordx2 v[204:205], %[p], off offset:2048 sc1\n"
      "global_load_dwordx2 v[206:207], %[p], off offset:3072 sc1\n"
      "s_sleep 2\n"
      "s_mov_b32 %[c], 0x10000\n"
      "GL_%=:\n"
      "global_load_dwordx2 v[208:209], %[p], off offset:0 sc1\n"
      "global_load_dwordx2 v[210:211], %[p], off offset:1024 sc1\n"
      "global_load_dwordx2 v[212:213], %[p], off offset:2048 sc1\n"
      "global_load_dwordx2 v[214:215], %[p], off offset:3072 sc1\n"
      "s_waitcnt vmcnt(4)\n"
      "v_xor_b32 %[t], %[tag], v201\n"
      "v_xor_b32 %[u], %[tag], v203\n"
      "v_or_b32 %[t], %[t], %[u]\n"
      "v_xor_b32 %[u], %[tag], v205\n"
      "v_or_b32 %[t], %[t], %[u]\n"
      "v_xor_b32 %[u], %[tag], v207\n"
      "v_or_b32 %[t], %[t], %[u]\n"
      "v_cmp_ne_u32 vcc, 0, %[t]\n"
      "s_nop 1\n"
      "s_cbranch_vccz G0_%=\n"
      "s_sleep 2\n"
      "global_load_dwordx2 v[200:201], %[p], off offset:0 sc1\n"
      "global_load_dwordx2 v[202:203], %[p], off offset:1024 sc1\n"
      "global_load_dwordx2 v[204:205], %[p], off offset:2048 sc1\n"
      "global_load_dwordx2 v[206:207], %[p], off offset:3072 sc1\n"
      "s_waitcnt vmcnt(4)\n"
      "v_xor_b32 %[t], %[tag], v209\n"
      "v_xor_b32 %[u], %[tag], v211\n"
      "v_or_b32 %[t], %[t], %[u]\n"
      "v_xor_b32 %[u], %[tag], v213\n"
      "v_or_b32 %[t], %[t], %[u]\n"
      "v_xor_b32 %[u], %[tag], v215\n"
      "v_or_b32 %[t], %[t], %[u]\n"
      "v_cmp_ne_u32 vcc, 0, %[t]\n"
      "s_nop 1\n"
      "s_cbranch_vccz G1_%=\n"
      "s_sleep 2\n"
      "s_sub_u32 %[c], %[c], 1\n"
      "s_cbranch_scc0 GL_%=\n"
      "G0_%=:\n"
      "v_mov_b32 %[o0], v200\n"
      "v_mov_b32 %[o1], v202\n"
      "v_mov_b32 %[o2], v204\n"
      "v_mov_b32 %[o3], v206\n"
      "s_branch GE_%=\n"
      "G1_%=:\n"
      "v_mov_b32 %[o0], v208\n"
      "v_mov_b32 %[o1], v210\n"
      "v_mov_b32 %[o2], v212\n"
      "v_mov_b32 %[o3], v214\n"
      "GE_%=:\n"
      : [o0] "=&v"(v[0]), [o1] "=&v"(v[1]), [o2] "=&v"(v[2]), [o3] "=&v"(v[3]),
        [t] "=&v"(t), [u] "=&v"(u), [c] "=&s"(c)
      : [p] "v"(p), [tag] "s"(tag)
      : "v200", "v201", "v202", "v203", "v204", "v205", "v206", "v207", "v208", "v209", "v210", "v211", "v212", "v213", "v214", "v215",
        "vcc", "scc", "memory");
}
__device__ __forceinline__ void poll8(const u64* p, unsigned tag, float (&v)[8]) {
  unsigned t, u, c;
  asm volatile(
      "global_load_dwordx2 v[200:201], %[p], off offset:0 sc1\n"
      "global_load_dwordx2 v[202:203], %[p], off offset:512 sc1\n"
      "global_load_dwordx2 v[204:205], %[p], off offset:1024 sc1\n"
      "global_load_dwordx2 v[206:207], %[p], off offset:1536 sc1\n"
      "global_load_dwordx2 v[208:209], %[p], off offset:2048 sc1\n"
      "global_load_dwordx2 v[210:211], %[p], off offset:2560 sc1\n"
      "global_load_dwordx2 v[212:213], %[p], off offset:3072 sc1\n"
      "global_load_dwordx2 v[214:215], %[p], off offset:3584 sc1\n"
      "s_sleep 2\n"
      "s_mov_b32 %[c], 0x10000\n"
      "GL_%=:\n"
      "global_load_dwordx2 v[216:217], %[p], off offset:0 sc1\n"
      "global_load_dwordx2 v[218:219], %[p], off offset:512 sc1\n"
      "global_load_dwordx2 v[220:221], %[p], off offset:1024 sc1\n"
      "global_load_dwordx2 v[222:223], %[p], off offset:1536 sc1\n"
      "global_load_dwordx2 v[224:225], %[p], off offset:2048 sc1\n"
      "global_load_dwordx2 v[226:227], %[p], off offset:2560 sc1\n"
      "global_load_dwordx2 v[228:229], %[p], off offset:3072 sc1\n"
      "global_load_dwordx2 v[230:231], %[p], off offset:3584 sc1\n"
      "s_waitcnt vmcnt(8)\n"
      "v_xor_b32 %[t], %[tag], v201\n"
      "v_xor_b32 %[u], %[tag], v203\n"
      "v_or_b32 %[t], %[t], %[u]\n"
      "v_xor_b32 %[u], %[tag], v205\n"
      "v_or_b32 %[t], %[t], %[u]\n"
      "v_xor_b32 %[u], %[tag], v207\n"
      "v_or_b32 %[t], %[t], %[u]\n"
      "v_xor_b32 %[u], %[tag], v209\n"
      "v_or_b32 %[t], %[t], %[u]\n"
      "v_xor_b32 %[u], %[tag], v211\n"
      "v_or_b32 %[t], %[t], %[u]\n"
      "v_xor_b32 %[u], %[tag], v213\n"
      "v_or_b32 %[t], %[t], %[u]\n"
      "v_xor_b32 %[u], %[tag], v215\n"
      "v_or_b32 %[t], %[t], %[u]\n"
      "v_cmp_ne_u32 vcc, 0, %[t]\n"
      "s_nop 1\n"
      "s_cbranch_vccz G0_%=\n"
      "s_sleep 2\n"
      "global_load_dwordx2 v[200:201], %[p], off offset:0 sc1\n"
      "global_load_dwordx2 v[202:203], %[p], off offset:512 sc1\n"
      "global_load_dwordx2 v[204:205], %[p], off offset:1024 sc1\n"
      "global_load_dwordx2 v[206:207], %[p], off offset:1536 sc1\n"
      "global_load_dwordx2 v[208:209], %[p], off offset:2048 sc1\n"
      "global_load_dwordx2 v[210:211], %[p], off offset:2560 sc1\n"
      "global_load_dwordx2 v[212:213], %[p], off offset:3072 sc1\n"
      "global_load_dwordx2 v[214:215], %[p], off offset:3584 sc1\n"
      "s_waitcnt vmcnt(8)\n"
      "v_xor_b32 %[t], %[tag], v217\n"
      "v_xor_b32 %[u], %[tag], v219\n"
      "v_or_b32 %[t], %[t], %[u]\n"
      "v_xor_b32 %[u], %[tag], v221\n"
      "v_or_b32 %[t], %[t], %[u]\n"
      "v_xor_b32 %[u], %[tag], v223\n"
      "v_or_b32 %[t], %[t], %[u]\n"
      "v_xor_b32 %[u], %[tag], v225\n"
      "v_or_b32 %[t], %[t], %[u]\n"
      "v_xor_b32 %[u], %[tag], v227\n"
      "v_or_b32 %[t], %[t], %[u]\n"
      "v_xor_b32 %[u], %[tag], v229\n"
      "v_or_b32 %[t], %[t], %[u]\n"
      "v_xor_b32 %[u], %[tag], v231\n"
      "v_or_b32 %[t], %[t], %[u]\n"
      "v_cmp_ne_u32 vcc, 0, %[t]\n"
      "s_nop 1\n"
      "s_cbranch_vccz G1_%=\n"
      "s_sleep 2\n"
      "s_sub_u32 %[c], %[c], 1\n"
      "s_cbranch_scc0 GL_%=\n"
      "G0_%=:\n"
      "v_mov_b32 %[o0], v200\n"
      "v_mov_b32 %[o1], v202\n"
      "v_mov_b32 %[o2], v204\n"
      "v_mov_b32 %[o3], v206\n"
      "v_mov_b32 %[o4], v208\n"
      "v_mov_b32 %[o5], v210\n"
      "v_mov_b32 %[o6], v212\n"
      "v_mov_b32 %[o7], v214\n"
      "s_branch GE_%=\n"
      "G1_%=:\n"
      "v_mov_b32 %[o0], v216\n"
      "v_mov_b32 %[o1], v218\n"
      "v_mov_b32 %[o2], v220\n"
      "v_mov_b32 %[o3], v222\n"
      "v_mov_b32 %[o4], v224\n"
      "v_mov_b32 %[o5], v226\n"
      "v_mov_b32 %[o6], v228\n"
      "v_mov_b32 %[o7], v230\n"
      "GE_%=:\n"
      : [o0] "=&v"(v[0]), [o1] "=&v"(v[1]), [o2] "=&v"(v[2]), [o3] "=&v"(v[3]), [o4] "=&v"(v[4]), [o5] "=&v"(v[5]), [o6] "=&v"(v[6]), [o7] "=&v"(v[7]),
        [t] "=&v"(t), [u] "=&v"(u), [c] "=&s"(c)
      : [p] "v"(p), [tag] "s"(tag)
      : "v200", "v201", "v202", "v203", "v204", "v205", "v206", "v207", "v208", "v209", "v210", "v211", "v212", "v213", "v214", "v215", "v216", "v217", "v218", "v219", "v220", "v221", "v222", "v223", "v224", "v225", "v226", "v227", "v228", "v229", "v230", "v231",
        "vcc", "scc", "memory");
}

__device__ __forceinline__ u32x4 ld16(const u16* p) { return __builtin_nontemporal_load((const u32x4*)p); }
__device__ __forceinline__ f32x4 mac4(f32x4 acc, unsigned s, u32x4 w, const u32x4 msk) {
  const u32x4 a = {s & msk[0], s & msk[1], s & msk[2], s & msk[3]};
  return __builtin_amdgcn_mfma_f32_16x16x32_bf16(__builtin_bit_cast(bf16x8, a), __builtin_bit_cast(bf16x8, w), acc, 0, 0, 0);
}
__device__ __forceinline__ unsigned lane_of(float v, int l) { return (unsigned)__builtin_amdgcn_readlane(__float_as_int(v), l); }
__device__ __forceinline__ unsigned lds_min4(unsigned a) {
  const u32x4 p = LDS(u32x4, a);
  return __builtin_amdgcn_readfirstlane(min(min(p[0], p[1]), min(p[2], p[3])));
}
template <int NAP> __device__ __forceinline__ void wait_min4(unsigned a, unsigned n) {
  for (unsigned it = 0; lds_min4(a) < n && it < SPIN_L; ++it) __builtin_amdgcn_s_sleep(NAP);
}
__device__ __forceinline__ float sum4(unsigned a) {
  return (LDS(float, a) + LDS(float, a + 512)) + (LDS(float, a + 1024) + LDS(float, a + 1536));
}

__global__ void __launch_bounds__(512, 2)
fused_decode(const u16* __restrict__ x, const char* __restrict__ Kc, const char* __restrict__ Vc, const u16* __restrict__ wq,
             const u16* __restrict__ wo, const u16* __restrict__ w1, const u16* __restrict__ w3, const u16* __restrict__ w2,
             u16* __restrict__ out, u64* __restrict__ ws) {
  __shared__ __attribute__((aligned(16))) char smem[L_BYTES];
  const int lane = threadIdx.x & 63, wv = __builtin_amdgcn_readfirstlane(threadIdx.x >> 6);
  const int b = blockIdx.x, h = b >> 3, m = b & 7, pc = m & 3, side = m >> 2;
  const unsigned lds0 = (unsigned)(uintptr_t)(LAS char*)smem;
  u64 tr;
  asm volatile("s_memtime %0" : "=s"(tr));
  asm volatile("s_waitcnt lgkmcnt(0)" : "+s"(tr) : "s"(x), "s"(Kc), "s"(Vc), "s"(wq), "s"(wo), "s"(w1), "s"(w3), "s"(w2), "s"(out), "s"(ws));

  const int rho = (pc >> 1) | ((pc & 1) << 2);
  const int hc = ((h >> 4) & 1) | (((h >> 3) & 1) << 2);
  const int rot = h & 15;
  const size_t hoff = (size_t)h << 23;
  const int j = wv - 4, r4 = lane >> 4, c = lane & 15;
  unsigned ox = 20 * lane, oy = 160 * m + 4 * (lane < 40 ? lane : 0), oq = (80 * m + 20 * j + r4) * (NH * DH * 2) + h * (DH * 2) + 16 * c;
  if (wv == 0) {
    const unsigned rnd = (unsigned)tr >> 2, po = ((rnd & 15u) << 15) | ((rnd & 0xf0u) << 4);
    const char* pp = ((h & 2) ? Vc : Kc) + hoff + ((rot == 8 ? 0 : 8 << 19) | ((rho ^ ((h & 1) << 1) ^ (h & 4) ^ hc) << 12));
    u64 t0, t1; unsigned pv, xcc;
    asm volatile(".p2align 6\n\tglobal_load_dword %3, %4, %5 nt\n\ts_memtime %0\n\ts_barrier\n\ts_waitcnt vmcnt(0)\n\ts_memtime %1\n\t"
                 "s_getreg_b32 %2, hwreg(HW_REG_XCC_ID)\n\ts_waitcnt lgkmcnt(0)"
                 : "=&s"(t0), "=&s"(t1), "=&s"(xcc), "=&v"(pv) : "v"(po), "s"(pp) : "memory");
    LDS(unsigned, lds0 + L_CNT + 8) = (min((unsigned)(t1 - t0), 4094u) + 1u) | ((xcc & 7u) << 16);
  } else {
    if (wv == 4 && lane < 8) LDS(unsigned, lds0 + L_CNT + lane * 4) = 0;
    asm volatile("s_waitcnt lgkmcnt(0)\n\ts_barrier" : "+v"(ox), "+v"(oy), "+v"(oq) :: "memory");
  }

  if (__builtin_expect(wv < 4, 0)) {
    const int rb = (rho | (side << 1)) ^ hc, r0 = rb ^ (wv & 2);
    const char* kb = Kc + hoff;
    const char* vb = Vc + hoff;
    asm volatile(
      "v_lshrrev_b32 v161, 4, %[lane]\n"
      "v_and_b32 v162, 15, %[lane]\n"
      "v_lshlrev_b32 v162, 4, v162\n"
      "v_lshl_or_b32 v162, v161, 15, v162\n"
      "s_lshl_b32 s46, %[wv], 2\n"
      "v_xor_b32 v161, s46, v161\n"
      "v_lshl_or_b32 v170, v161, 8, v162\n"
      "v_xor_b32 v171, 0x20400, v170\n"
      "v_xor_b32 v172, 0x100, v170\n"
      "v_xor_b32 v173, 0x20500, v170\n"
      "s_mov_b32 s45, %[rot]\n"
      "s_bfe_u32 s47, s45, 0x20001\n"
      "s_and_b32 s56, s45, 1\n"
      "s_lshl2_add_u32 s47, s56, s47\n"
      "s_xor_b32 s47, s47, %[r0]\n"
      "s_lshl_b32 s47, s47, 12\n"
      "s_lshl_b32 s56, s45, 19\n"
      "s_or_b32 s47, s47, s56\n"
      "s_mov_b64 s[40:41], %[kb]\n"
      "s_add_u32 s40, s40, s47\n"
      "s_addc_u32 s41, s41, 0\n"
      "s_cmp_eq_u32 %[wv], 0\n"
      "s_cbranch_scc1 LQ_%=\n"
      "s_sleep 1\n"
      "LQ_%=:\n"
      "global_load_dwordx4 v[64:67], v170, s[40:41] nt\n"
      "global_load_dwordx4 v[68:71], v171, s[40:41] nt\n"
      "global_load_dwordx4 v[72:75], v172, s[40:41] nt\n"
      "global_load_dwordx4 v[76:79], v173, s[40:41] nt\n"
      "s_load_dword s66, %[tp0], 0\n"
      "s_load_dword s66, %[tp1], 0\n"
      "s_load_dword s66, %[tp2], 0\n"
      "s_setprio 2\n"
      "v_xor_b32 v174, 0x200, v170\n"
      "v_xor_b32 v175, 0x20600, v170\n"
      "v_xor_b32 v176, 0x300, v170\n"
      "v_xor_b32 v177, 0x20700, v170\n"
      "v_xor_b32 v178, 0x40000, v170\n"
      "v_xor_b32 v179, 0x60400, v170\n"
      "v_xor_b32 v180, 0x40100, v170\n"
      "v_xor_b32 v181, 0x60500, v170\n"
      "v_xor_b32 v182, 0x40200, v170\n"
      "v_xor_b32 v183, 0x60600, v170\n"
      "v_xor_b32 v184, 0x40300, v170\n"
      "v_xor_b32 v185, 0x60700, v170\n"
      "s_mov_b32 s48, 0x11111111\n"
      "s_mov_b32 s49, 0x11111111\n"
      "s_mov_b32 s50, 0x22222222\n"
      "s_mov_b32 s51, 0x22222222\n"
      "s_mov_b32 s52, 0x44444444\n"
      "s_mov_b32 s53, 0x44444444\n"
      "s_mov_b32 s54, 0x88888888\n"
      "s_mov_b32 s55, 0x88888888\n"
      "v_and_b32 v161, 15, %[lane]\n"
      "v_and_b32 v162, 1, %[lane]\n"
      "v_lshlrev_b32 v162, 4, v162\n"
      "v_mov_b32 v163, 0xffff\n"
      "v_lshlrev_b32 v162, v162, v163\n"
      "v_lshrrev_b32 v161, 1, v161\n"
      "v_cmp_eq_u32 vcc, 0, v161\n"
      "s_nop 1\n"
      "v_cndmask_b32 v144, 0, v162, vcc\n"
      "v_cmp_eq_u32 vcc, 1, v161\n"
      "s_nop 1\n"
      "v_cndmask_b32 v145, 0, v162, vcc\n"
      "v_cmp_eq_u32 vcc, 2, v161\n"
      "s_nop 1\n"
      "v_cndmask_b32 v146, 0, v162, vcc\n"
      "v_cmp_eq_u32 vcc, 3, v161\n"
      "s_nop 1\n"
      "v_cndmask_b32 v147, 0, v162, vcc\n"
      "v_mov_b32 v165, %[lpo]\n"
      "v_mov_b32 v161, 0\n"
      "s_mov_b64 exec, 1\n"
      "ds_write_b32 v165, v161\n"
      "ds_write_b32 v165, v161 offset:16\n"
      "ds_write_b32 v165, v161 offset:32\n"
      "v_mov_b32 v156, 0xff800000\n"
      "ds_write_b32 v165, v156 offset:48\n"
      "s_mov_b64 exec, -1\n"
      "v_mov_b32 v152, 0\n"
      "v_mov_b32 v153, 0\n"
      "v_mov_b32 v154, 0\n"
      "v_mov_b32 v155, 0\n"
      "v_mov_b32 v156, 0xff800000\n"
      "v_mov_b32 v143, 0\n"
      "v_lshlrev_b32 v164, 2, %[lane]\n"
      "v_add_u32 v164, %[lso], v164\n"
      "v_and_b32 v161, 15, %[lane]\n"
      "v_lshlrev_b32 v161, 4, v161\n"
      "v_add_u32 v161, %[lq], v161\n"
      "s_mov_b32 s44, 0\n"
      "s_mov_b32 s59, 0\n"
      "s_waitcnt vmcnt(3)\n"
      "global_load_dwordx4 v[80:83], v174, s[40:41] nt\n"
      "s_waitcnt vmcnt(3)\n"
      "global_load_dwordx4 v[84:87], v175, s[40:41] nt\n"
      "s_waitcnt vmcnt(3)\n"
      "global_load_dwordx4 v[88:91], v176, s[40:41] nt\n"
      "s_waitcnt vmcnt(3)\n"
      "global_load_dwordx4 v[92:95], v177, s[40:41] nt\n"
      "s_waitcnt vmcnt(3)\n"
      "global_load_dwordx4 v[96:99], v178, s[40:41] nt\n"
      "s_waitcnt vmcnt(3)\n"
      "global_load_dwordx4 v[100:103], v179, s[40:41] nt\n"
      "s_waitcnt vmcnt(3)\n"
      "global_load_dwordx4 v[104:107], v180, s[40:41] nt\n"
      "s_waitcnt vmcnt(3)\n"
      "global_load_dwordx4 v[108:111], v181, s[40:41] nt\n"
      "s_mov_b32 s58, 0x100000\n"
      "LD_%=:\n"
      "v_mov_b32 v162, %[lq]\n"
      "ds_read_b32 v162, v162 offset:324\n"
      "s_waitcnt lgkmcnt(0)\n"
      "v_readfirstlane_b32 s78, v162\n"
      "s_cmp_lg_u32 s78, 0\n"
      "s_cbranch_scc1 LF_%=\n"
      "s_sub_u32 s58, s58, 1\n"
      "s_cbranch_scc0 LD_%=\n"
      "LF_%=:\n"
      "s_and_b32 s68, s78, 7\n"
      "s_bfe_u32 s69, s78, 0x30004\n"
      "s_xor_b32 s68, s68, %[rb]\n"
      "s_xor_b32 s69, s69, %[rb]\n"
      "s_bfe_u32 s78, s78, 0x10008\n"
      "s_bfe_u32 s47, s45, 0x20001\n"
      "s_and_b32 s56, s45, 1\n"
      "s_lshl2_add_u32 s47, s56, s47\n"
      "s_xor_b32 s47, s47, s68\n"
      "s_lshl_b32 s47, s47, 12\n"
      "s_lshl_b32 s56, s45, 19\n"
      "s_or_b32 s47, s47, s56\n"
      "s_mov_b64 s[40:41], %[kb]\n"
      "s_add_u32 s40, s40, s47\n"
      "s_addc_u32 s41, s41, 0\n"
      "s_waitcnt vmcnt(3)\n"
      "global_load_dwordx4 v[112:115], v182, s[40:41] nt\n"
      "s_waitcnt vmcnt(3)\n"
      "global_load_dwordx4 v[116:119], v183, s[40:41] nt\n"
      "s_waitcnt vmcnt(3)\n"
      "global_load_dwordx4 v[120:123], v184, s[40:41] nt\n"
      "s_waitcnt vmcnt(3)\n"
      "global_load_dwordx4 v[124:127], v185, s[40:41] nt\n"
      "s_waitcnt lgkmcnt(0)\n"
      "s_barrier\n"
      "ds_read_b128 v[128:131], v161\n"
      "s_waitcnt lgkmcnt(0)\n"
      "s_add_i32 s45, s45, 1\n"
      "s_and_b32 s45, s45, 15\n"
      "s_add_i32 s44, s44, 1\n"
      "s_cmp_eq_u32 s44, 16\n"
      "s_cselect_b32 s46, s69, s68\n"
      "s_cselect_b64 s[42:43], %[vb], %[kb]\n"
      "s_bfe_u32 s47, s45, 0x20001\n"
      "s_and_b32 s56, s45, 1\n"
      "s_lshl2_add_u32 s47, s56, s47\n"
      "s_xor_b32 s47, s47, s46\n"
      "s_lshl_b32 s47, s47, 12\n"
      "s_lshl_b32 s56, s45, 19\n"
      "s_or_b32 s47, s47, s56\n"
      "s_add_u32 s42, s42, s47\n"
      "s_addc_u32 s43, s43, 0\n"
      "s_waitcnt vmcnt(15)\n"
      "v_dot2_f32_bf16 v136, v64, v128, 0\n"
      "v_dot2_f32_bf16 v136, v65, v129, v136\n"
      "v_dot2_f32_bf16 v136, v66, v130, v136\n"
      "v_dot2_f32_bf16 v136, v67, v131, v136\n"
      "s_waitcnt vmcnt(14)\n"
      "global_load_dwordx4 v[64:67], v170, s[42:43] nt\n"
      "v_dot2_f32_bf16 v137, v68, v128, 0\n"
      "v_dot2_f32_bf16 v137, v69, v129, v137\n"
      "v_dot2_f32_bf16 v137, v70, v130, v137\n"
      "v_dot2_f32_bf16 v137, v71, v131, v137\n"
      "s_waitcnt vmcnt(14)\n"
      "global_load_dwordx4 v[68:71], v171, s[42:43] nt\n"
      "v_dot2_f32_bf16 v138, v72, v128, 0\n"
      "v_dot2_f32_bf16 v138, v73, v129, v138\n"
      "v_dot2_f32_bf16 v138, v74, v130, v138\n"
      "v_dot2_f32_bf16 v138, v75, v131, v138\n"
      "s_waitcnt vmcnt(14)\n"
      "global_load_dwordx4 v[72:75], v172, s[42:43] nt\n"
      "v_dot2_f32_bf16 v139, v76, v128, 0\n"
      "v_dot2_f32_bf16 v139, v77, v129, v139\n"
      "v_dot2_f32_bf16 v139, v78, v130, v139\n"
      "v_dot2_f32_bf16 v139, v79, v131, v139\n"
      "v_add_f32_dpp v140, v136, v136 row_ror:8 row_mask:0xf bank_mask:0x3\n"
      "v_add_f32_dpp v141, v137, v137 row_ror:8 row_mask:0xf bank_mask:0x3\n"
      "s_nop 0\n"
      "v_add_f32_dpp v140, v138, v138 row_ror:8 row_mask:0xf bank_mask:0xc\n"
      "v_add_f32_dpp v141, v139, v139 row_ror:8 row_mask:0xf bank_mask:0xc\n"
      "s_nop 0\n"
      "v_add_f32_dpp v142, v140, v140 row_half_mirror row_mask:0xf bank_mask:0x5\n"
      "s_nop 1\n"
      "v_add_f32_dpp v142, v141, v141 row_half_mirror row_mask:0xf bank_mask:0xa\n"
      "s_nop 1\n"
      "v_add_f32_dpp v142, v142, v142 quad_perm:[2,3,0,1] row_mask:0xf bank_mask:0xf\n"
      "s_nop 1\n"
      "v_add_f32_dpp v142, v142, v142 quad_perm:[1,0,3,2] row_mask:0xf bank_mask:0xf\n"
      "v_cndmask_b32 v143, v143, v142, s[48:49]\n"
      "s_waitcnt vmcnt(14)\n"
      "global_load_dwordx4 v[76:79], v173, s[42:43] nt\n"
      "v_dot2_f32_bf16 v136, v80, v128, 0\n"
      "v_dot2_f32_bf16 v136, v81, v129, v136\n"
      "v_dot2_f32_bf16 v136, v82, v130, v136\n"
      "v_dot2_f32_bf16 v136, v83, v131, v136\n"
      "s_waitcnt vmcnt(14)\n"
      "global_load_dwordx4 v[80:83], v174, s[42:43] nt\n"
      "v_dot2_f32_bf16 v137, v84, v128, 0\n"
      "v_dot2_f32_bf16 v137, v85, v129, v137\n"
      "v_dot2_f32_bf16 v137, v86, v130, v137\n"
      "v_dot2_f32_bf16 v137, v87, v131, v137\n"
      "s_waitcnt vmcnt(14)\n"
      "v_dot2_f32_bf16 v138, v88, v128, 0\n"
      "v_dot2_f32_bf16 v138, v89, v129, v138\n"
      "v_dot2_f32_bf16 v138, v90, v130, v138\n"
      "v_dot2_f32_bf16 v138, v91, v131, v138\n"
      "s_waitcnt vmcnt(13)\n"
      "v_dot2_f32_bf16 v139, v92, v128, 0\n"
      "v_dot2_f32_bf16 v139, v93, v129, v139\n"
      "v_dot2_f32_bf16 v139, v94, v130, v139\n"
      "v_dot2_f32_bf16 v139, v95, v131, v139\n"
      "v_add_f32_dpp v140, v136, v136 row_ror:8 row_mask:0xf bank_mask:0x3\n"
      "v_add_f32_dpp v141, v137, v137 row_ror:8 row_mask:0xf bank_mask:0x3\n"
      "s_nop 0\n"
      "v_add_f32_dpp v140, v138, v138 row_ror:8 row_mask:0xf bank_mask:0xc\n"
      "v_add_f32_dpp v141, v139, v139 row_ror:8 row_mask:0xf bank_mask:0xc\n"
      "s_nop 0\n"
      "v_add_f32_dpp v142, v140, v140 row_half_mirror row_mask:0xf bank_mask:0x5\n"
      "s_nop 1\n"
      "v_add_f32_dpp v142, v141, v141 row_half_mirror row_mask:0xf bank_mask:0xa\n"
      "s_nop 1\n"
      "v_add_f32_dpp v142, v142, v142 quad_perm:[2,3,0,1] row_mask:0xf bank_mask:0xf\n"
      "s_nop 1\n"
      "v_add_f32_dpp v142, v142, v142 quad_perm:[1,0,3,2] row_mask:0xf bank_mask:0xf\n"
      "v_cndmask_b32 v143, v143, v142, s[50:51]\n"
      "s_waitcnt vmcnt(12)\n"
      "v_dot2_f32_bf16 v136, v96, v128, 0\n"
      "v_dot2_f32_bf16 v136, v97, v129, v136\n"
      "v_dot2_f32_bf16 v136, v98, v130, v136\n"
      "v_dot2_f32_bf16 v136, v99, v131, v136\n"
      "s_waitcnt vmcnt(11)\n"
      "v_dot2_f32_bf16 v137, v100, v128, 0\n"
      "v_dot2_f32_bf16 v137, v101, v129, v137\n"
      "v_dot2_f32_bf16 v137, v102, v130, v137\n"
      "v_dot2_f32_bf16 v137, v103, v131, v137\n"
      "s_waitcnt vmcnt(10)\n"
      "v_dot2_f32_bf16 v138, v104, v128, 0\n"
      "v_dot2_f32_bf16 v138, v105, v129, v138\n"
      "v_dot2_f32_bf16 v138, v106, v130, v138\n"
      "v_dot2_f32_bf16 v138, v107, v131, v138\n"
      "s_waitcnt vmcnt(9)\n"
      "v_dot2_f32_bf16 v139, v108, v128, 0\n"
      "v_dot2_f32_bf16 v139, v109, v129, v139\n"
      "v_dot2_f32_bf16 v139, v110, v130, v139\n"
      "v_dot2_f32_bf16 v139, v111, v131, v139\n"
      "v_add_f32_dpp v140, v136, v136 row_ror:8 row_mask:0xf bank_mask:0x3\n"
      "v_add_f32_dpp v141, v137, v137 row_ror:8 row_mask:0xf bank_mask:0x3\n"
      "s_nop 0\n"
      "v_add_f32_dpp v140, v138, v138 row_ror:8 row_mask:0xf bank_mask:0xc\n"
      "v_add_f32_dpp v141, v139, v139 row_ror:8 row_mask:0xf bank_mask:0xc\n"
      "s_nop 0\n"
      "v_add_f32_dpp v142, v140, v140 row_half_mirror row_mask:0xf bank_mask:0x5\n"
      "s_nop 1\n"
      "v_add_f32_dpp v142, v141, v141 row_half_mirror row_mask:0xf bank_mask:0xa\n"
      "s_nop 1\n"
      "v_add_f32_dpp v142, v142, v142 quad_perm:[2,3,0,1] row_mask:0xf bank_mask:0xf\n"
      "s_nop 1\n"
      "v_add_f32_dpp v142, v142, v142 quad_perm:[1,0,3,2] row_mask:0xf bank_mask:0xf\n"
      "v_cndmask_b32 v143, v143, v142, s[52:53]\n"
      "s_waitcnt vmcnt(8)\n"
      "v_dot2_f32_bf16 v136, v112, v128, 0\n"
      "v_dot2_f32_bf16 v136, v113, v129, v136\n"
      "v_dot2_f32_bf16 v136, v114, v130, v136\n"
      "v_dot2_f32_bf16 v136, v115, v131, v136\n"
      "s_waitcnt vmcnt(7)\n"
      "v_dot2_f32_bf16 v137, v116, v128, 0\n"
      "v_dot2_f32_bf16 v137, v117, v129, v137\n"
      "v_dot2_f32_bf16 v137, v118, v130, v137\n"
      "v_dot2_f32_bf16 v137, v119, v131, v137\n"
      "s_waitcnt vmcnt(6)\n"
      "v_dot2_f32_bf16 v138, v120, v128, 0\n"
      "v_dot2_f32_bf16 v138, v121, v129, v138\n"
      "v_dot2_f32_bf16 v138, v122, v130, v138\n"
      "v_dot2_f32_bf16 v138, v123, v131, v138\n"
      "s_waitcnt vmcnt(5)\n"
      "v_dot2_f32_bf16 v139, v124, v128, 0\n"
      "v_dot2_f32_bf16 v139, v125, v129, v139\n"
      "v_dot2_f32_bf16 v139, v126, v130, v139\n"
      "v_dot2_f32_bf16 v139, v127, v131, v139\n"
      "v_add_f32_dpp v140, v136, v136 row_ror:8 row_mask:0xf bank_mask:0x3\n"
      "v_add_f32_dpp v141, v137, v137 row_ror:8 row_mask:0xf bank_mask:0x3\n"
      "s_nop 0\n"
      "v_add_f32_dpp v140, v138, v138 row_ror:8 row_mask:0xf bank_mask:0xc\n"
      "v_add_f32_dpp v141, v139, v139 row_ror:8 row_mask:0xf bank_mask:0xc\n"
      "s_nop 0\n"
      "v_add_f32_dpp v142, v140, v140 row_half_mirror row_mask:0xf bank_mask:0x5\n"
      "s_nop 1\n"
      "v_add_f32_dpp v142, v141, v141 row_half_mirror row_mask:0xf bank_mask:0xa\n"
      "s_nop 1\n"
      "v_add_f32_dpp v142, v142, v142 quad_perm:[2,3,0,1] row_mask:0xf bank_mask:0xf\n"
      "s_nop 1\n"
      "v_add_f32_dpp v142, v142, v142 quad_perm:[1,0,3,2] row_mask:0xf bank_mask:0xf\n"
      "v_cndmask_b32 v143, v143, v142, s[54:55]\n"
      "v_cvt_pk_bf16_f32 v161, v143, v143\n"
      "v_lshlrev_b32 v161, 16, v161\n"
      "v_mul_f32 v161, 0x3db504f3, v161\n"
      "v_cvt_pk_bf16_f32 v161, v161, v161\n"
      "v_lshlrev_b32 v161, 16, v161\n"
      "ds_write_b32 v164, v161\n"
      "v_mov_b32 v162, s44\n"
      "s_mov_b64 exec, 1\n"
      "ds_write_b32 v165, v162\n"
      "s_mov_b64 exec, -1\n"
      "v_add_u32 v164, 0x100, v164\n"
      "s_mov_b64 s[40:41], s[42:43]\n"
      "LK_%=:\n"
      "s_add_i32 s45, s45, 1\n"
      "s_and_b32 s45, s45, 15\n"
      "s_add_i32 s44, s44, 1\n"
      "s_cmp_eq_u32 s44, 16\n"
      "s_cselect_b32 s46, s69, s68\n"
      "s_cselect_b64 s[42:43], %[vb], %[kb]\n"
      "s_bfe_u32 s47, s45, 0x20001\n"
      "s_and_b32 s56, s45, 1\n"
      "s_lshl2_add_u32 s47, s56, s47\n"
      "s_xor_b32 s47, s47, s46\n"
      "s_lshl_b32 s47, s47, 12\n"
      "s_lshl_b32 s56, s45, 19\n"
      "s_or_b32 s47, s47, s56\n"
      "s_add_u32 s42, s42, s47\n"
      "s_addc_u32 s43, s43, 0\n"
      "s_waitcnt vmcnt(4)\n"
      "global_load_dwordx4 v[84:87], v175, s[40:41] nt\n"
      "v_dot2_f32_bf16 v136, v64, v128, 0\n"
      "v_dot2_f32_bf16 v136, v65, v129, v136\n"
      "v_dot2_f32_bf16 v136, v66, v130, v136\n"
      "v_dot2_f32_bf16 v136, v67, v131, v136\n"
      "s_waitcnt vmcnt(4)\n"
      "global_load_dwordx4 v[88:91], v176, s[40:41] nt\n"
      "v_dot2_f32_bf16 v137, v68, v128, 0\n"
      "v_dot2_f32_bf16 v137, v69, v129, v137\n"
      "v_dot2_f32_bf16 v137, v70, v130, v137\n"
      "v_dot2_f32_bf16 v137, v71, v131, v137\n"
      "s_waitcnt vmcnt(4)\n"
      "global_load_dwordx4 v[92:95], v177, s[40:41] nt\n"
      "v_dot2_f32_bf16 v138, v72, v128, 0\n"
      "v_dot2_f32_bf16 v138, v73, v129, v138\n"
      "v_dot2_f32_bf16 v138, v74, v130, v138\n"
      "v_dot2_f32_bf16 v138, v75, v131, v138\n"
      "s_waitcnt vmcnt(4)\n"
      "global_load_dwordx4 v[96:99], v178, s[40:41] nt\n"
      "v_dot2_f32_bf16 v139, v76, v128, 0\n"
      "v_dot2_f32_bf16 v139, v77, v129, v139\n"
      "v_dot2_f32_bf16 v139, v78, v130, v139\n"
      "v_dot2_f32_bf16 v139, v79, v131, v139\n"
      "v_add_f32_dpp v140, v136, v136 row_ror:8 row_mask:0xf bank_mask:0x3\n"
      "v_add_f32_dpp v141, v137, v137 row_ror:8 row_mask:0xf bank_mask:0x3\n"
      "s_nop 0\n"
      "v_add_f32_dpp v140, v138, v138 row_ror:8 row_mask:0xf bank_mask:0xc\n"
      "v_add_f32_dpp v141, v139, v139 row_ror:8 row_mask:0xf bank_mask:0xc\n"
      "s_nop 0\n"
      "v_add_f32_dpp v142, v140, v140 row_half_mirror row_mask:0xf bank_mask:0x5\n"
      "s_nop 1\n"
      "v_add_f32_dpp v142, v141, v141 row_half_mirror row_mask:0xf bank_mask:0xa\n"
      "s_nop 1\n"
      "v_add_f32_dpp v142, v142, v142 quad_perm:[2,3,0,1] row_mask:0xf bank_mask:0xf\n"
      "s_nop 1\n"
      "v_add_f32_dpp v142, v142, v142 quad_perm:[1,0,3,2] row_mask:0xf bank_mask:0xf\n"
      "v_cndmask_b32 v143, v143, v142, s[48:49]\n"
      "s_waitcnt vmcnt(4)\n"
      "global_load_dwordx4 v[100:103], v179, s[40:41] nt\n"
      "v_dot2_f32_bf16 v136, v80, v128, 0\n"
      "v_dot2_f32_bf16 v136, v81, v129, v136\n"
      "v_dot2_f32_bf16 v136, v82, v130, v136\n"
      "v_dot2_f32_bf16 v136, v83, v131, v136\n"
      "s_waitcnt vmcnt(4)\n"
      "global_load_dwordx4 v[104:107], v180, s[40:41] nt\n"
      "v_dot2_f32_bf16 v137, v84, v128, 0\n"
      "v_dot2_f32_bf16 v137, v85, v129, v137\n"
      "v_dot2_f32_bf16 v137, v86, v130, v137\n"
      "v_dot2_f32_bf16 v137, v87, v131, v137\n"
      "s_waitcnt vmcnt(4)\n"
      "global_load_dwordx4 v[108:111], v181, s[40:41] nt\n"
      "v_dot2_f32_bf16 v138, v88, v128, 0\n"
      "v_dot2_f32_bf16 v138, v89, v129, v138\n"
      "v_dot2_f32_bf16 v138, v90, v130, v138\n"
      "v_dot2_f32_bf16 v138, v91, v131, v138\n"
      "s_waitcnt vmcnt(4)\n"
      "global_load_dwordx4 v[112:115], v182, s[40:41] nt\n"
      "v_dot2_f32_bf16 v139, v92, v128, 0\n"
      "v_dot2_f32_bf16 v139, v93, v129, v139\n"
      "v_dot2_f32_bf16 v139, v94, v130, v139\n"
      "v_dot2_f32_bf16 v139, v95, v131, v139\n"
      "v_add_f32_dpp v140, v136, v136 row_ror:8 row_mask:0xf bank_mask:0x3\n"
      "v_add_f32_dpp v141, v137, v137 row_ror:8 row_mask:0xf bank_mask:0x3\n"
      "s_nop 0\n"
      "v_add_f32_dpp v140, v138, v138 row_ror:8 row_mask:0xf bank_mask:0xc\n"
      "v_add_f32_dpp v141, v139, v139 row_ror:8 row_mask:0xf bank_mask:0xc\n"
      "s_nop 0\n"
      "v_add_f32_dpp v142, v140, v140 row_half_mirror row_mask:0xf bank_mask:0x5\n"
      "s_nop 1\n"
      "v_add_f32_dpp v142, v141, v141 row_half_mirror row_mask:0xf bank_mask:0xa\n"
      "s_nop 1\n"
      "v_add_f32_dpp v142, v142, v142 quad_perm:[2,3,0,1] row_mask:0xf bank_mask:0xf\n"
      "s_nop 1\n"
      "v_add_f32_dpp v142, v142, v142 quad_perm:[1,0,3,2] row_mask:0xf bank_mask:0xf\n"
      "v_cndmask_b32 v143, v143, v142, s[50:51]\n"
      "s_waitcnt vmcnt(4)\n"
      "global_load_dwordx4 v[116:119], v183, s[40:41] nt\n"
      "v_dot2_f32_bf16 v136, v96, v128, 0\n"
      "v_dot2_f32_bf16 v136, v97, v129, v136\n"
      "v_dot2_f32_bf16 v136, v98, v130, v136\n"
      "v_dot2_f32_bf16 v136, v99, v131, v136\n"
      "s_waitcnt vmcnt(4)\n"
      "global_load_dwordx4 v[120:123], v184, s[40:41] nt\n"
      "v_dot2_f32_bf16 v137, v100, v128, 0\n"
      "v_dot2_f32_bf16 v137, v101, v129, v137\n"
      "v_dot2_f32_bf16 v137, v102, v130, v137\n"
      "v_dot2_f32_bf16 v137, v103, v131, v137\n"
      "s_waitcnt vmcnt(4)\n"
      "global_load_dwordx4 v[124:127], v185, s[40:41] nt\n"
      "v_dot2_f32_bf16 v138, v104, v128, 0\n"
      "v_dot2_f32_bf16 v138, v105, v129, v138\n"
      "v_dot2_f32_bf16 v138, v106, v130, v138\n"
      "v_dot2_f32_bf16 v138, v107, v131, v138\n"
      "s_waitcnt vmcnt(4)\n"
      "global_load_dwordx4 v[64:67], v170, s[42:43] nt\n"
      "v_dot2_f32_bf16 v139, v108, v128, 0\n"
      "v_dot2_f32_bf16 v139, v109, v129, v139\n"
      "v_dot2_f32_bf16 v139, v110, v130, v139\n"
      "v_dot2_f32_bf16 v139, v111, v131, v139\n"
      "v_add_f32_dpp v140, v136, v136 row_ror:8 row_mask:0xf bank_mask:0x3\n"
      "v_add_f32_dpp v141, v137, v137 row_ror:8 row_mask:0xf bank_mask:0x3\n"
      "s_nop 0\n"
      "v_add_f32_dpp v140, v138, v138 row_ror:8 row_mask:0xf bank_mask:0xc\n"
      "v_add_f32_dpp v141, v139, v139 row_ror:8 row_mask:0xf bank_mask:0xc\n"
      "s_nop 0\n"
      "v_add_f32_dpp v142, v140, v140 row_half_mirror row_mask:0xf bank_mask:0x5\n"
      "s_nop 1\n"
      "v_add_f32_dpp v142, v141, v141 row_half_mirror row_mask:0xf bank_mask:0xa\n"
      "s_nop 1\n"
      "v_add_f32_dpp v142, v142, v142 quad_perm:[2,3,0,1] row_mask:0xf bank_mask:0xf\n"
      "s_nop 1\n"
      "v_add_f32_dpp v142, v142, v142 quad_perm:[1,0,3,2] row_mask:0xf bank_mask:0xf\n"
      "v_cndmask_b32 v143, v143, v142, s[52:53]\n"
      "s_waitcnt vmcnt(4)\n"
      "global_load_dwordx4 v[68:71], v171, s[42:43] nt\n"
      "v_dot2_f32_bf16 v136, v112, v128, 0\n"
      "v_dot2_f32_bf16 v136, v113, v129, v136\n"
      "v_dot2_f32_bf16 v136, v114, v130, v136\n"
      "v_dot2_f32_bf16 v136, v115, v131, v136\n"
      "s_waitcnt vmcnt(4)\n"
      "global_load_dwordx4 v[72:75], v172, s[42:43] nt\n"
      "v_dot2_f32_bf16 v137, v116, v128, 0\n"
      "v_dot2_f32_bf16 v137, v117, v129, v137\n"
      "v_dot2_f32_bf16 v137, v118, v130, v137\n"
      "v_dot2_f32_bf16 v137, v119, v131, v137\n"
      "s_waitcnt vmcnt(4)\n"
      "global_load_dwordx4 v[76:79], v173, s[42:43] nt\n"
      "v_dot2_f32_bf16 v138, v120, v128, 0\n"
      "v_dot2_f32_bf16 v138, v121, v129, v138\n"
      "v_dot2_f32_bf16 v138, v122, v130, v138\n"
      "v_dot2_f32_bf16 v138, v123, v131, v138\n"
      "s_waitcnt vmcnt(4)\n"
      "global_load_dwordx4 v[80:83], v174, s[42:43] nt\n"
      "v_dot2_f32_bf16 v139, v124, v128, 0\n"
      "v_dot2_f32_bf16 v139, v125, v129, v139\n"
      "v_dot2_f32_bf16 v139, v126, v130, v139\n"
      "v_dot2_f32_bf16 v139, v127, v131, v139\n"
      "v_add_f32_dpp v140, v136, v136 row_ror:8 row_mask:0xf bank_mask:0x3\n"
      "v_add_f32_dpp v141, v137, v137 row_ror:8 row_mask:0xf bank_mask:0x3\n"
      "s_nop 0\n"
      "v_add_f32_dpp v140, v138, v138 row_ror:8 row_mask:0xf bank_mask:0xc\n"
      "v_add_f32_dpp v141, v139, v139 row_ror:8 row_mask:0xf bank_mask:0xc\n"
      "s_nop 0\n"
      "v_add_f32_dpp v142, v140, v140 row_half_mirror row_mask:0xf bank_mask:0x5\n"
      "s_nop 1\n"
      "v_add_f32_dpp v142, v141, v141 row_half_mirror row_mask:0xf bank_mask:0xa\n"
      "s_nop 1\n"
      "v_add_f32_dpp v142, v142, v142 quad_perm:[2,3,0,1] row_mask:0xf bank_mask:0xf\n"
      "s_nop 1\n"
      "v_add_f32_dpp v142, v142, v142 quad_perm:[1,0,3,2] row_mask:0xf bank_mask:0xf\n"
      "v_cndmask_b32 v143, v143, v142, s[54:55]\n"
      "v_cvt_pk_bf16_f32 v161, v143, v143\n"
      "v_lshlrev_b32 v161, 16, v161\n"
      "v_mul_f32 v161, 0x3db504f3, v161\n"
      "v_cvt_pk_bf16_f32 v161, v161, v161\n"
      "v_lshlrev_b32 v161, 16, v161\n"
      "ds_write_b32 v164, v161\n"
      "v_mov_b32 v162, s44\n"
      "s_mov_b64 exec, 1\n"
      "ds_write_b32 v165, v162\n"
      "s_mov_b64 exec, -1\n"
      "v_add_u32 v164, 0x100, v164\n"
      "s_mov_b64 s[40:41], s[42:43]\n"
      "s_cmp_lt_u32 s44, 16\n"
      "s_cbranch_scc1 LK_%=\n"
      "v_lshlrev_b32 v164, 2, %[lane]\n"
      "s_lshl_b32 s46, s78, 14\n"
      "s_add_u32 s46, s46, %[lso]\n"
      "v_add_u32 v164, s46, v164\n"
      "v_add_u32 v169, 16, v165\n"
      "s_mov_b32 s44, 0\n"
      "ds_read_b32 v159, v169\n"
      "ds_read_b32 v168, v165 offset:32\n"
      "ds_read_b32 v157, v165 offset:48\n"
      "ds_read_b32 v158, v164\n"
      "LV_%=:\n"
      "s_add_i32 s45, s45, 1\n"
      "s_and_b32 s45, s45, 15\n"
      "s_bfe_u32 s47, s45, 0x20001\n"
      "s_and_b32 s56, s45, 1\n"
      "s_lshl2_add_u32 s47, s56, s47\n"
      "s_xor_b32 s47, s47, s69\n"
      "s_lshl_b32 s47, s47, 12\n"
      "s_lshl_b32 s56, s45, 19\n"
      "s_or_b32 s47, s47, s56\n"
      "s_mov_b64 s[42:43], %[vb]\n"
      "s_add_u32 s42, s42, s47\n"
      "s_addc_u32 s43, s43, 0\n"
      "s_add_i32 s44, s44, 1\n"
      "s_mov_b32 s58, 0x100000\n"
      "LCa_%=:\n"
      "s_waitcnt lgkmcnt(0)\n"
      "v_readfirstlane_b32 s46, v159\n"
      "v_readfirstlane_b32 s57, v168\n"
      "s_cmp_ge_u32 s46, s44\n"
      "s_cbranch_scc1 LRa_%=\n"
      "s_sub_u32 s58, s58, 1\n"
      "s_cbranch_scc1 LRa_%=\n"
      "s_sleep 1\n"
      "ds_read_b32 v159, v169\n"
      "ds_read_b32 v168, v165 offset:32\n"
      "ds_read_b32 v157, v165 offset:48\n"
      "ds_read_b32 v158, v164\n"
      "s_branch LCa_%=\n"
      "LRa_%=:\n"
      "s_cmp_lt_u32 s44, s57\n"
      "s_cbranch_scc1 LBv_%=\n"
      "s_waitcnt vmcnt(4)\n"
      "s_cmp_lg_u32 s59, 0\n"
      "s_cbranch_scc1 LS0_%=\n"
      "global_load_dwordx4 v[84:87], v175, s[40:41] nt\n"
      "LS0_%=:\n"
      "v_max_f32_dpp v161, v158, v158 row_ror:8 row_mask:0xf bank_mask:0xf\n"
      "s_nop 1\n"
      "v_max_f32_dpp v161, v161, v161 row_ror:4 row_mask:0xf bank_mask:0xf\n"
      "s_nop 1\n"
      "v_max_f32_dpp v161, v161, v161 row_ror:2 row_mask:0xf bank_mask:0xf\n"
      "s_nop 1\n"
      "v_max_f32_dpp v161, v161, v161 row_ror:1 row_mask:0xf bank_mask:0xf\n"
      "s_nop 1\n"
      "v_max_f32_dpp v161, v161, v161 row_bcast:15 row_mask:0xa bank_mask:0xf\n"
      "s_nop 1\n"
      "v_max_f32_dpp v161, v161, v161 row_bcast:31 row_mask:0xc bank_mask:0xf\n"
      "s_nop 0\n"
      "v_readlane_b32 s57, v161, 63\n"
      "s_nop 1\n"
      "v_max_f32 v163, s57, v156\n"
      "v_sub_f32 v161, v156, v163\n"
      "v_sub_f32 v162, v158, v163\n"
      "v_mul_f32 v161, 0x3fb8aa3b, v161\n"
      "v_mul_f32 v162, 0x3fb8aa3b, v162\n"
      "v_exp_f32 v166, v161\n"
      "v_exp_f32 v162, v162\n"
      "v_mov_b32 v156, v163\n"
      "v_add_u32 v164, 0x100, v164\n"
      "v_cvt_pk_bf16_f32 v160, v162, v162\n"
      "v_mul_f32 v152, v166, v152\n"
      "v_mul_f32 v153, v166, v153\n"
      "v_mul_f32 v154, v166, v154\n"
      "v_mul_f32 v155, v166, v155\n"
      "v_and_b32_dpp v148, v160, v144 row_newbcast:0 row_mask:0xf bank_mask:0xf\n"
      "v_and_b32_dpp v149, v160, v145 row_newbcast:0 row_mask:0xf bank_mask:0xf\n"
      "v_and_b32_dpp v150, v160, v146 row_newbcast:0 row_mask:0xf bank_mask:0xf\n"
      "v_and_b32_dpp v151, v160, v147 row_newbcast:0 row_mask:0xf bank_mask:0xf\n"
      "s_nop 1\n"
      "v_mfma_f32_16x16x32_bf16 v[152:155], v[148:151], v[64:67], v[152:155]\n"
      "s_waitcnt vmcnt(4)\n"
      "s_cmp_lg_u32 s59, 0\n"
      "s_cbranch_scc1 LS1_%=\n"
      "global_load_dwordx4 v[88:91], v176, s[40:41] nt\n"
      "LS1_%=:\n"
      "v_and_b32_dpp v148, v160, v144 row_newbcast:4 row_mask:0xf bank_mask:0xf\n"
      "v_and_b32_dpp v149, v160, v145 row_newbcast:4 row_mask:0xf bank_mask:0xf\n"
      "v_and_b32_dpp v150, v160, v146 row_newbcast:4 row_mask:0xf bank_mask:0xf\n"
      "v_and_b32_dpp v151, v160, v147 row_newbcast:4 row_mask:0xf bank_mask:0xf\n"
      "s_nop 1\n"
      "v_mfma_f32_16x16x32_bf16 v[152:155], v[148:151], v[68:71], v[152:155]\n"
      "s_waitcnt vmcnt(4)\n"
      "global_load_dwordx4 v[92:95], v177, s[40:41] nt\n"
      "v_and_b32_dpp v148, v160, v144 row_newbcast:8 row_mask:0xf bank_mask:0xf\n"
      "v_and_b32_dpp v149, v160, v145 row_newbcast:8 row_mask:0xf bank_mask:0xf\n"
      "v_and_b32_dpp v150, v160, v146 row_newbcast:8 row_mask:0xf bank_mask:0xf\n"
      "v_and_b32_dpp v151, v160, v147 row_newbcast:8 row_mask:0xf bank_mask:0xf\n"
      "s_nop 1\n"
      "v_mfma_f32_16x16x32_bf16 v[152:155], v[148:151], v[72:75], v[152:155]\n"
      "s_waitcnt vmcnt(4)\n"
      "global_load_dwordx4 v[96:99], v178, s[40:41] nt\n"
      "v_and_b32_dpp v148, v160, v144 row_newbcast:12 row_mask:0xf bank_mask:0xf\n"
      "v_and_b32_dpp v149, v160, v145 row_newbcast:12 row_mask:0xf bank_mask:0xf\n"
      "v_and_b32_dpp v150, v160, v146 row_newbcast:12 row_mask:0xf bank_mask:0xf\n"
      "v_and_b32_dpp v151, v160, v147 row_newbcast:12 row_mask:0xf bank_mask:0xf\n"
      "s_nop 1\n"
      "v_mfma_f32_16x16x32_bf16 v[152:155], v[148:151], v[76:79], v[152:155]\n"
      "s_waitcnt vmcnt(4)\n"
      "global_load_dwordx4 v[100:103], v179, s[40:41] nt\n"
      "v_and_b32_dpp v148, v160, v144 row_newbcast:1 row_mask:0xf bank_mask:0xf\n"
      "v_and_b32_dpp v149, v160, v145 row_newbcast:1 row_mask:0xf bank_mask:0xf\n"
      "v_and_b32_dpp v150, v160, v146 row_newbcast:1 row_mask:0xf bank_mask:0xf\n"
      "v_and_b32_dpp v151, v160, v147 row_newbcast:1 row_mask:0xf bank_mask:0xf\n"
      "s_nop 1\n"
      "v_mfma_f32_16x16x32_bf16 v[152:155], v[148:151], v[80:83], v[152:155]\n"
      "s_waitcnt vmcnt(4)\n"
      "global_load_dwordx4 v[104:107], v180, s[40:41] nt\n"
      "v_and_b32_dpp v148, v160, v144 row_newbcast:5 row_mask:0xf bank_mask:0xf\n"
      "v_and_b32_dpp v149, v160, v145 row_newbcast:5 row_mask:0xf bank_mask:0xf\n"
      "v_and_b32_dpp v150, v160, v146 row_newbcast:5 row_mask:0xf bank_mask:0xf\n"
      "v_and_b32_dpp v151, v160, v147 row_newbcast:5 row_mask:0xf bank_mask:0xf\n"
      "s_nop 1\n"
      "v_mfma_f32_16x16x32_bf16 v[152:155], v[148:151], v[84:87], v[152:155]\n"
      "s_waitcnt vmcnt(4)\n"
      "global_load_dwordx4 v[108:111], v181, s[40:41] nt\n"
      "v_and_b32_dpp v148, v160, v144 row_newbcast:9 row_mask:0xf bank_mask:0xf\n"
      "v_and_b32_dpp v149, v160, v145 row_newbcast:9 row_mask:0xf bank_mask:0xf\n"
      "v_and_b32_dpp v150, v160, v146 row_newbcast:9 row_mask:0xf bank_mask:0xf\n"
      "v_and_b32_dpp v151, v160, v147 row_newbcast:9 row_mask:0xf bank_mask:0xf\n"
      "s_nop 1\n"
      "v_mfma_f32_16x16x32_bf16 v[152:155], v[148:151], v[88:91], v[152:155]\n"
      "s_waitcnt vmcnt(4)\n"
      "global_load_dwordx4 v[112:115], v182, s[40:41] nt\n"
      "v_and_b32_dpp v148, v160, v144 row_newbcast:13 row_mask:0xf bank_mask:0xf\n"
      "v_and_b32_dpp v149, v160, v145 row_newbcast:13 row_mask:0xf bank_mask:0xf\n"
      "v_and_b32_dpp v150, v160, v146 row_newbcast:13 row_mask:0xf bank_mask:0xf\n"
      "v_and_b32_dpp v151, v160, v147 row_newbcast:13 row_mask:0xf bank_mask:0xf\n"
      "s_nop 1\n"
      "v_mfma_f32_16x16x32_bf16 v[152:155], v[148:151], v[92:95], v[152:155]\n"
      "s_waitcnt vmcnt(4)\n"
      "global_load_dwordx4 v[116:119], v183, s[40:41] nt\n"
      "v_and_b32_dpp v148, v160, v144 row_newbcast:2 row_mask:0xf bank_mask:0xf\n"
      "v_and_b32_dpp v149, v160, v145 row_newbcast:2 row_mask:0xf bank_mask:0xf\n"
      "v_and_b32_dpp v150, v160, v146 row_newbcast:2 row_mask:0xf bank_mask:0xf\n"
      "v_and_b32_dpp v151, v160, v147 row_newbcast:2 row_mask:0xf bank_mask:0xf\n"
      "s_nop 1\n"
      "v_mfma_f32_16x16x32_bf16 v[152:155], v[148:151], v[96:99], v[152:155]\n"
      "s_waitcnt vmcnt(4)\n"
      "global_load_dwordx4 v[120:123], v184, s[40:41] nt\n"
      "v_and_b32_dpp v148, v160, v144 row_newbcast:6 row_mask:0xf bank_mask:0xf\n"
      "v_and_b32_dpp v149, v160, v145 row_newbcast:6 row_mask:0xf bank_mask:0xf\n"
      "v_and_b32_dpp v150, v160, v146 row_newbcast:6 row_mask:0xf bank_mask:0xf\n"
      "v_and_b32_dpp v151, v160, v147 row_newbcast:6 row_mask:0xf bank_mask:0xf\n"
      "s_nop 1\n"
      "v_mfma_f32_16x16x32_bf16 v[152:155], v[148:151], v[100:103], v[152:155]\n"
      "s_waitcnt vmcnt(4)\n"
      "global_load_dwordx4 v[124:127], v185, s[40:41] nt\n"
      "v_and_b32_dpp v148, v160, v144 row_newbcast:10 row_mask:0xf bank_mask:0xf\n"
      "v_and_b32_dpp v149, v160, v145 row_newbcast:10 row_mask:0xf bank_mask:0xf\n"
      "v_and_b32_dpp v150, v160, v146 row_newbcast:10 row_mask:0xf bank_mask:0xf\n"
      "v_and_b32_dpp v151, v160, v147 row_newbcast:10 row_mask:0xf bank_mask:0xf\n"
      "s_nop 1\n"
      "v_mfma_f32_16x16x32_bf16 v[152:155], v[148:151], v[104:107], v[152:155]\n"
      "s_waitcnt vmcnt(4)\n"
      "global_load_dwordx4 v[64:67], v170, s[42:43] nt\n"
      "v_and_b32_dpp v148, v160, v144 row_newbcast:14 row_mask:0xf bank_mask:0xf\n"
      "v_and_b32_dpp v149, v160, v145 row_newbcast:14 row_mask:0xf bank_mask:0xf\n"
      "v_and_b32_dpp v150, v160, v146 row_newbcast:14 row_mask:0xf bank_mask:0xf\n"
      "v_and_b32_dpp v151, v160, v147 row_newbcast:14 row_mask:0xf bank_mask:0xf\n"
      "s_nop 1\n"
      "v_mfma_f32_16x16x32_bf16 v[152:155], v[148:151], v[108:111], v[152:155]\n"
      "s_waitcnt vmcnt(4)\n"
      "global_load_dwordx4 v[68:71], v171, s[42:43] nt\n"
      "v_and_b32_dpp v148, v160, v144 row_newbcast:3 row_mask:0xf bank_mask:0xf\n"
      "v_and_b32_dpp v149, v160, v145 row_newbcast:3 row_mask:0xf bank_mask:0xf\n"
      "v_and_b32_dpp v150, v160, v146 row_newbcast:3 row_mask:0xf bank_mask:0xf\n"
      "v_and_b32_dpp v151, v160, v147 row_newbcast:3 row_mask:0xf bank_mask:0xf\n"
      "s_nop 1\n"
      "v_mfma_f32_16x16x32_bf16 v[152:155], v[148:151], v[112:115], v[152:155]\n"
      "s_waitcnt vmcnt(4)\n"
      "global_load_dwordx4 v[72:75], v172, s[42:43] nt\n"
      "v_and_b32_dpp v148, v160, v144 row_newbcast:7 row_mask:0xf bank_mask:0xf\n"
      "v_and_b32_dpp v149, v160, v145 row_newbcast:7 row_mask:0xf bank_mask:0xf\n"
      "v_and_b32_dpp v150, v160, v146 row_newbcast:7 row_mask:0xf bank_mask:0xf\n"
      "v_and_b32_dpp v151, v160, v147 row_newbcast:7 row_mask:0xf bank_mask:0xf\n"
      "s_nop 1\n"
      "v_mfma_f32_16x16x32_bf16 v[152:155], v[148:151], v[116:119], v[152:155]\n"
      "s_waitcnt vmcnt(4)\n"
      "global_load_dwordx4 v[76:79], v173, s[42:43] nt\n"
      "v_and_b32_dpp v148, v160, v144 row_newbcast:11 row_mask:0xf bank_mask:0xf\n"
      "v_and_b32_dpp v149, v160, v145 row_newbcast:11 row_mask:0xf bank_mask:0xf\n"
      "v_and_b32_dpp v150, v160, v146 row_newbcast:11 row_mask:0xf bank_mask:0xf\n"
      "v_and_b32_dpp v151, v160, v147 row_newbcast:11 row_mask:0xf bank_mask:0xf\n"
      "s_nop 1\n"
      "v_mfma_f32_16x16x32_bf16 v[152:155], v[148:151], v[120:123], v[152:155]\n"
      "ds_read_b32 v159, v169\n"
      "ds_read_b32 v168, v165 offset:32\n"
      "ds_read_b32 v157, v165 offset:48\n"
      "ds_read_b32 v158, v164\n"
      "s_waitcnt vmcnt(4)\n"
      "global_load_dwordx4 v[80:83], v174, s[42:43] nt\n"
      "v_and_b32_dpp v148, v160, v144 row_newbcast:15 row_mask:0xf bank_mask:0xf\n"
      "v_and_b32_dpp v149, v160, v145 row_newbcast:15 row_mask:0xf bank_mask:0xf\n"
      "v_and_b32_dpp v150, v160, v146 row_newbcast:15 row_mask:0xf bank_mask:0xf\n"
      "v_and_b32_dpp v151, v160, v147 row_newbcast:15 row_mask:0xf bank_mask:0xf\n"
      "s_nop 1\n"
      "v_mfma_f32_16x16x32_bf16 v[152:155], v[148:151], v[124:127], v[152:155]\n"
      "s_mov_b32 s59, 0\n"
      "LT_%=:\n"
      "s_add_i32 s46, s44, 16\n"
      "v_mov_b32 v162, s46\n"
      "s_mov_b64 exec, 1\n"
      "ds_write_b32 v165, v162\n"
      "s_mov_b64 exec, -1\n"
      "s_mov_b64 s[40:41], s[42:43]\n"
      "s_cmp_lt_u32 s44, 15\n"
      "s_cbranch_scc1 LV_%=\n"
      "s_add_i32 s44, s44, 1\n"
      "s_mov_b32 s58, 0x100000\n"
      "LCb_%=:\n"
      "s_waitcnt lgkmcnt(0)\n"
      "v_readfirstlane_b32 s46, v159\n"
      "v_readfirstlane_b32 s57, v157\n"
      "s_cmp_eq_u32 s57, 0xff800000\n"
      "s_cselect_b32 s46, 0, s46\n"
      "s_cmp_ge_u32 s46, s44\n"
      "s_cbranch_scc1 LRb_%=\n"
      "s_sub_u32 s58, s58, 1\n"
      "s_cbranch_scc1 LRb_%=\n"
      "s_sleep 1\n"
      "ds_read_b32 v159, v169\n"
      "ds_read_b32 v168, v165 offset:32\n"
      "ds_read_b32 v157, v165 offset:48\n"
      "ds_read_b32 v158, v164\n"
      "s_branch LCb_%=\n"
      "LRb_%=:\n"
      "s_cmp_lg_u32 s59, 0\n"
      "s_cbranch_scc1 LX_%=\n"
      "global_load_dwordx4 v[84:87], v175, s[40:41] nt\n"
      "global_load_dwordx4 v[88:91], v176, s[40:41] nt\n"
      "LX_%=:\n"
      "global_load_dwordx4 v[92:95], v177, s[40:41] nt\n"
      "s_waitcnt vmcnt(7)\n"
      "global_load_dwordx4 v[96:99], v178, s[40:41] nt\n"
      "v_max_f32_dpp v161, v158, v158 row_ror:8 row_mask:0xf bank_mask:0xf\n"
      "s_nop 1\n"
      "v_max_f32_dpp v161, v161, v161 row_ror:4 row_mask:0xf bank_mask:0xf\n"
      "s_nop 1\n"
      "v_max_f32_dpp v161, v161, v161 row_ror:2 row_mask:0xf bank_mask:0xf\n"
      "s_nop 1\n"
      "v_max_f32_dpp v161, v161, v161 row_ror:1 row_mask:0xf bank_mask:0xf\n"
      "s_nop 1\n"
      "v_max_f32_dpp v161, v161, v161 row_bcast:15 row_mask:0xa bank_mask:0xf\n"
      "s_nop 1\n"
      "v_max_f32_dpp v161, v161, v161 row_bcast:31 row_mask:0xc bank_mask:0xf\n"
      "s_nop 0\n"
      "v_readlane_b32 s57, v161, 63\n"
      "s_nop 1\n"
      "v_max3_f32 v163, s57, v156, v157\n"
      "v_sub_f32 v161, v156, v163\n"
      "v_sub_f32 v162, v158, v163\n"
      "v_mul_f32 v161, 0x3fb8aa3b, v161\n"
      "v_mul_f32 v162, 0x3fb8aa3b, v162\n"
      "v_exp_f32 v166, v161\n"
      "v_exp_f32 v162, v162\n"
      "v_mov_b32 v156, v163\n"
      "v_add_u32 v164, 0x100, v164\n"
      "v_cvt_pk_bf16_f32 v160, v162, v162\n"
      "v_mul_f32 v152, v166, v152\n"
      "v_mul_f32 v153, v166, v153\n"
      "v_mul_f32 v154, v166, v154\n"
      "v_mul_f32 v155, v166, v155\n"
      "v_and_b32 v161, 15, %[lane]\n"
      "v_lshlrev_b32 v161, 5, v161\n"
      "v_and_b32 v162, 16, %[lane]\n"
      "v_add3_u32 v164, v161, v162, %[lout]\n"
      "v_and_b32_dpp v148, v160, v144 row_newbcast:0 row_mask:0xf bank_mask:0xf\n"
      "v_and_b32_dpp v149, v160, v145 row_newbcast:0 row_mask:0xf bank_mask:0xf\n"
      "v_and_b32_dpp v150, v160, v146 row_newbcast:0 row_mask:0xf bank_mask:0xf\n"
      "v_and_b32_dpp v151, v160, v147 row_newbcast:0 row_mask:0xf bank_mask:0xf\n"
      "s_nop 1\n"
      "v_mfma_f32_16x16x32_bf16 v[152:155], v[148:151], v[64:67], v[152:155]\n"
      "s_waitcnt vmcnt(7)\n"
      "global_load_dwordx4 v[100:103], v179, s[40:41] nt\n"
      "v_and_b32_dpp v148, v160, v144 row_newbcast:4 row_mask:0xf bank_mask:0xf\n"
      "v_and_b32_dpp v149, v160, v145 row_newbcast:4 row_mask:0xf bank_mask:0xf\n"
      "v_and_b32_dpp v150, v160, v146 row_newbcast:4 row_mask:0xf bank_mask:0xf\n"
      "v_and_b32_dpp v151, v160, v147 row_newbcast:4 row_mask:0xf bank_mask:0xf\n"
      "s_nop 1\n"
      "v_mfma_f32_16x16x32_bf16 v[152:155], v[148:151], v[68:71], v[152:155]\n"
      "s_waitcnt vmcnt(7)\n"
      "global_load_dwordx4 v[104:107], v180, s[40:41] nt\n"
      "v_and_b32_dpp v148, v160, v144 row_newbcast:8 row_mask:0xf bank_mask:0xf\n"
      "v_and_b32_dpp v149, v160, v145 row_newbcast:8 row_mask:0xf bank_mask:0xf\n"
      "v_and_b32_dpp v150, v160, v146 row_newbcast:8 row_mask:0xf bank_mask:0xf\n"
      "v_and_b32_dpp v151, v160, v147 row_newbcast:8 row_mask:0xf bank_mask:0xf\n"
      "s_nop 1\n"
      "v_mfma_f32_16x16x32_bf16 v[152:155], v[148:151], v[72:75], v[152:155]\n"
      "s_waitcnt vmcnt(7)\n"
      "global_load_dwordx4 v[108:111], v181, s[40:41] nt\n"
      "v_and_b32_dpp v148, v160, v144 row_newbcast:12 row_mask:0xf bank_mask:0xf\n"
      "v_and_b32_dpp v149, v160, v145 row_newbcast:12 row_mask:0xf bank_mask:0xf\n"
      "v_and_b32_dpp v150, v160, v146 row_newbcast:12 row_mask:0xf bank_mask:0xf\n"
      "v_and_b32_dpp v151, v160, v147 row_newbcast:12 row_mask:0xf bank_mask:0xf\n"
      "s_nop 1\n"
      "v_mfma_f32_16x16x32_bf16 v[152:155], v[148:151], v[76:79], v[152:155]\n"
      "s_waitcnt vmcnt(7)\n"
      "global_load_dwordx4 v[112:115], v182, s[40:41] nt\n"
      "v_and_b32_dpp v148, v160, v144 row_newbcast:1 row_mask:0xf bank_mask:0xf\n"
      "v_and_b32_dpp v149, v160, v145 row_newbcast:1 row_mask:0xf bank_mask:0xf\n"
      "v_and_b32_dpp v150, v160, v146 row_newbcast:1 row_mask:0xf bank_mask:0xf\n"
      "v_and_b32_dpp v151, v160, v147 row_newbcast:1 row_mask:0xf bank_mask:0xf\n"
      "s_nop 1\n"
      "v_mfma_f32_16x16x32_bf16 v[152:155], v[148:151], v[80:83], v[152:155]\n"
      "s_waitcnt vmcnt(7)\n"
      "global_load_dwordx4 v[116:119], v183, s[40:41] nt\n"
      "v_and_b32_dpp v148, v160, v144 row_newbcast:5 row_mask:0xf bank_mask:0xf\n"
      "v_and_b32_dpp v149, v160, v145 row_newbcast:5 row_mask:0xf bank_mask:0xf\n"
      "v_and_b32_dpp v150, v160, v146 row_newbcast:5 row_mask:0xf bank_mask:0xf\n"
      "v_and_b32_dpp v151, v160, v147 row_newbcast:5 row_mask:0xf bank_mask:0xf\n"
      "s_nop 1\n"
      "v_mfma_f32_16x16x32_bf16 v[152:155], v[148:151], v[84:87], v[152:155]\n"
      "s_waitcnt vmcnt(7)\n"
      "global_load_dwordx4 v[120:123], v184, s[40:41] nt\n"
      "v_and_b32_dpp v148, v160, v144 row_newbcast:9 row_mask:0xf bank_mask:0xf\n"
      "v_and_b32_dpp v149, v160, v145 row_newbcast:9 row_mask:0xf bank_mask:0xf\n"
      "v_and_b32_dpp v150, v160, v146 row_newbcast:9 row_mask:0xf bank_mask:0xf\n"
      "v_and_b32_dpp v151, v160, v147 row_newbcast:9 row_mask:0xf bank_mask:0xf\n"
      "s_nop 1\n"
      "v_mfma_f32_16x16x32_bf16 v[152:155], v[148:151], v[88:91], v[152:155]\n"
      "s_waitcnt vmcnt(7)\n"
      "global_load_dwordx4 v[124:127], v185, s[40:41] nt\n"
      "v_and_b32_dpp v148, v160, v144 row_newbcast:13 row_mask:0xf bank_mask:0xf\n"
      "v_and_b32_dpp v149, v160, v145 row_newbcast:13 row_mask:0xf bank_mask:0xf\n"
      "v_and_b32_dpp v150, v160, v146 row_newbcast:13 row_mask:0xf bank_mask:0xf\n"
      "v_and_b32_dpp v151, v160, v147 row_newbcast:13 row_mask:0xf bank_mask:0xf\n"
      "s_nop 1\n"
      "v_mfma_f32_16x16x32_bf16 v[152:155], v[148:151], v[92:95], v[152:155]\n"
      "s_waitcnt vmcnt(7)\n"
      "v_and_b32_dpp v148, v160, v144 row_newbcast:2 row_mask:0xf bank_mask:0xf\n"
      "v_and_b32_dpp v149, v160, v145 row_newbcast:2 row_mask:0xf bank_mask:0xf\n"
      "v_and_b32_dpp v150, v160, v146 row_newbcast:2 row_mask:0xf bank_mask:0xf\n"
      "v_and_b32_dpp v151, v160, v147 row_newbcast:2 row_mask:0xf bank_mask:0xf\n"
      "s_nop 1\n"
      "v_mfma_f32_16x16x32_bf16 v[152:155], v[148:151], v[96:99], v[152:155]\n"
      "s_waitcnt vmcnt(6)\n"
      "v_and_b32_dpp v148, v160, v144 row_newbcast:6 row_mask:0xf bank_mask:0xf\n"
      "v_and_b32_dpp v149, v160, v145 row_newbcast:6 row_mask:0xf bank_mask:0xf\n"
      "v_and_b32_dpp v150, v160, v146 row_newbcast:6 row_mask:0xf bank_mask:0xf\n"
      "v_and_b32_dpp v151, v160, v147 row_newbcast:6 row_mask:0xf bank_mask:0xf\n"
      "s_nop 1\n"
      "v_mfma_f32_16x16x32_bf16 v[152:155], v[148:151], v[100:103], v[152:155]\n"
      "s_waitcnt vmcnt(5)\n"
      "v_and_b32_dpp v148, v160, v144 row_newbcast:10 row_mask:0xf bank_mask:0xf\n"
      "v_and_b32_dpp v149, v160, v145 row_newbcast:10 row_mask:0xf bank_mask:0xf\n"
      "v_and_b32_dpp v150, v160, v146 row_newbcast:10 row_mask:0xf bank_mask:0xf\n"
      "v_and_b32_dpp v151, v160, v147 row_newbcast:10 row_mask:0xf bank_mask:0xf\n"
      "s_nop 1\n"
      "v_mfma_f32_16x16x32_bf16 v[152:155], v[148:151], v[104:107], v[152:155]\n"
      "s_waitcnt vmcnt(4)\n"
      "v_and_b32_dpp v148, v160, v144 row_newbcast:14 row_mask:0xf bank_mask:0xf\n"
      "v_and_b32_dpp v149, v160, v145 row_newbcast:14 row_mask:0xf bank_mask:0xf\n"
      "v_and_b32_dpp v150, v160, v146 row_newbcast:14 row_mask:0xf bank_mask:0xf\n"
      "v_and_b32_dpp v151, v160, v147 row_newbcast:14 row_mask:0xf bank_mask:0xf\n"
      "s_nop 1\n"
      "v_mfma_f32_16x16x32_bf16 v[152:155], v[148:151], v[108:111], v[152:155]\n"
      "s_waitcnt vmcnt(3)\n"
      "v_and_b32_dpp v148, v160, v144 row_newbcast:3 row_mask:0xf bank_mask:0xf\n"
      "v_and_b32_dpp v149, v160, v145 row_newbcast:3 row_mask:0xf bank_mask:0xf\n"
      "v_and_b32_dpp v150, v160, v146 row_newbcast:3 row_mask:0xf bank_mask:0xf\n"
      "v_and_b32_dpp v151, v160, v147 row_newbcast:3 row_mask:0xf bank_mask:0xf\n"
      "s_nop 1\n"
      "v_mfma_f32_16x16x32_bf16 v[152:155], v[148:151], v[112:115], v[152:155]\n"
      "s_waitcnt vmcnt(2)\n"
      "v_and_b32_dpp v148, v160, v144 row_newbcast:7 row_mask:0xf bank_mask:0xf\n"
      "v_and_b32_dpp v149, v160, v145 row_newbcast:7 row_mask:0xf bank_mask:0xf\n"
      "v_and_b32_dpp v150, v160, v146 row_newbcast:7 row_mask:0xf bank_mask:0xf\n"
      "v_and_b32_dpp v151, v160, v147 row_newbcast:7 row_mask:0xf bank_mask:0xf\n"
      "s_nop 1\n"
      "v_mfma_f32_16x16x32_bf16 v[152:155], v[148:151], v[116:119], v[152:155]\n"
      "s_waitcnt vmcnt(1)\n"
      "v_and_b32_dpp v148, v160, v144 row_newbcast:11 row_mask:0xf bank_mask:0xf\n"
      "v_and_b32_dpp v149, v160, v145 row_newbcast:11 row_mask:0xf bank_mask:0xf\n"
      "v_and_b32_dpp v150, v160, v146 row_newbcast:11 row_mask:0xf bank_mask:0xf\n"
      "v_and_b32_dpp v151, v160, v147 row_newbcast:11 row_mask:0xf bank_mask:0xf\n"
      "s_nop 1\n"
      "v_mfma_f32_16x16x32_bf16 v[152:155], v[148:151], v[120:123], v[152:155]\n"
      "s_waitcnt vmcnt(0)\n"
      "v_and_b32_dpp v148, v160, v144 row_newbcast:15 row_mask:0xf bank_mask:0xf\n"
      "v_and_b32_dpp v149, v160, v145 row_newbcast:15 row_mask:0xf bank_mask:0xf\n"
      "v_and_b32_dpp v150, v160, v146 row_newbcast:15 row_mask:0xf bank_mask:0xf\n"
      "v_and_b32_dpp v151, v160, v147 row_newbcast:15 row_mask:0xf bank_mask:0xf\n"
      "s_nop 1\n"
      "v_mfma_f32_16x16x32_bf16 v[152:155], v[148:151], v[124:127], v[152:155]\n"
      "s_nop 7\n"
      "s_mov_b32 exec_hi, 0\n"
      "ds_write_b128 v164, v[152:155]\n"
      "s_mov_b64 exec, -1\n"
      "s_waitcnt lgkmcnt(0)\n"
      "s_barrier\n"
      "s_branch LE_%=\n"
      "LBv_%=:\n"
      "s_cmp_lg_u32 s59, 0\n"
      "s_cbranch_scc1 LY_%=\n"
      "global_load_dwordx4 v[84:87], v175, s[40:41] nt\n"
      "global_load_dwordx4 v[88:91], v176, s[40:41] nt\n"
      "LY_%=:\n"
      "s_waitcnt vmcnt(6)\n"
      "global_load_dwordx4 v[92:95], v177, s[40:41] nt\n"
      "v_max_f32_dpp v161, v158, v158 row_ror:8 row_mask:0xf bank_mask:0xf\n"
      "s_nop 1\n"
      "v_max_f32_dpp v161, v161, v161 row_ror:4 row_mask:0xf bank_mask:0xf\n"
      "s_nop 1\n"
      "v_max_f32_dpp v161, v161, v161 row_ror:2 row_mask:0xf bank_mask:0xf\n"
      "s_nop 1\n"
      "v_max_f32_dpp v161, v161, v161 row_ror:1 row_mask:0xf bank_mask:0xf\n"
      "s_nop 1\n"
      "v_max_f32_dpp v161, v161, v161 row_bcast:15 row_mask:0xa bank_mask:0xf\n"
      "s_nop 1\n"
      "v_max_f32_dpp v161, v161, v161 row_bcast:31 row_mask:0xc bank_mask:0xf\n"
      "s_nop 0\n"
      "v_readlane_b32 s57, v161, 63\n"
      "s_nop 1\n"
      "v_max_f32 v163, s57, v156\n"
      "v_sub_f32 v161, v156, v163\n"
      "v_sub_f32 v162, v158, v163\n"
      "v_mul_f32 v161, 0x3fb8aa3b, v161\n"
      "v_mul_f32 v162, 0x3fb8aa3b, v162\n"
      "v_exp_f32 v166, v161\n"
      "v_exp_f32 v162, v162\n"
      "v_mov_b32 v156, v163\n"
      "v_add_u32 v164, 0x100, v164\n"
      "v_cvt_pk_bf16_f32 v160, v162, v162\n"
      "v_mul_f32 v152, v166, v152\n"
      "v_mul_f32 v153, v166, v153\n"
      "v_mul_f32 v154, v166, v154\n"
      "v_mul_f32 v155, v166, v155\n"
      "v_and_b32_dpp v148, v160, v144 row_newbcast:0 row_mask:0xf bank_mask:0xf\n"
      "v_and_b32_dpp v149, v160, v145 row_newbcast:0 row_mask:0xf bank_mask:0xf\n"
      "v_and_b32_dpp v150, v160, v146 row_newbcast:0 row_mask:0xf bank_mask:0xf\n"
      "v_and_b32_dpp v151, v160, v147 row_newbcast:0 row_mask:0xf bank_mask:0xf\n"
      "s_nop 1\n"
      "v_mfma_f32_16x16x32_bf16 v[152:155], v[148:151], v[64:67], v[152:155]\n"
      "s_waitcnt vmcnt(6)\n"
      "global_load_dwordx4 v[96:99], v178, s[40:41] nt\n"
      "v_and_b32_dpp v148, v160, v144 row_newbcast:4 row_mask:0xf bank_mask:0xf\n"
      "v_and_b32_dpp v149, v160, v145 row_newbcast:4 row_mask:0xf bank_mask:0xf\n"
      "v_and_b32_dpp v150, v160, v146 row_newbcast:4 row_mask:0xf bank_mask:0xf\n"
      "v_and_b32_dpp v151, v160, v147 row_newbcast:4 row_mask:0xf bank_mask:0xf\n"
      "s_nop 1\n"
      "v_mfma_f32_16x16x32_bf16 v[152:155], v[148:151], v[68:71], v[152:155]\n"
      "s_waitcnt vmcnt(6)\n"
      "global_load_dwordx4 v[100:103], v179, s[40:41] nt\n"
      "v_and_b32_dpp v148, v160, v144 row_newbcast:8 row_mask:0xf bank_mask:0xf\n"
      "v_and_b32_dpp v149, v160, v145 row_newbcast:8 row_mask:0xf bank_mask:0xf\n"
      "v_and_b32_dpp v150, v160, v146 row_newbcast:8 row_mask:0xf bank_mask:0xf\n"
      "v_and_b32_dpp v151, v160, v147 row_newbcast:8 row_mask:0xf bank_mask:0xf\n"
      "s_nop 1\n"
      "v_mfma_f32_16x16x32_bf16 v[152:155], v[148:151], v[72:75], v[152:155]\n"
      "s_waitcnt vmcnt(6)\n"
      "global_load_dwordx4 v[104:107], v180, s[40:41] nt\n"
      "v_and_b32_dpp v148, v160, v144 row_newbcast:12 row_mask:0xf bank_mask:0xf\n"
      "v_and_b32_dpp v149, v160, v145 row_newbcast:12 row_mask:0xf bank_mask:0xf\n"
      "v_and_b32_dpp v150, v160, v146 row_newbcast:12 row_mask:0xf bank_mask:0xf\n"
      "v_and_b32_dpp v151, v160, v147 row_newbcast:12 row_mask:0xf bank_mask:0xf\n"
      "s_nop 1\n"
      "v_mfma_f32_16x16x32_bf16 v[152:155], v[148:151], v[76:79], v[152:155]\n"
      "s_waitcnt vmcnt(6)\n"
      "global_load_dwordx4 v[108:111], v181, s[40:41] nt\n"
      "v_and_b32_dpp v148, v160, v144 row_newbcast:1 row_mask:0xf bank_mask:0xf\n"
      "v_and_b32_dpp v149, v160, v145 row_newbcast:1 row_mask:0xf bank_mask:0xf\n"
      "v_and_b32_dpp v150, v160, v146 row_newbcast:1 row_mask:0xf bank_mask:0xf\n"
      "v_and_b32_dpp v151, v160, v147 row_newbcast:1 row_mask:0xf bank_mask:0xf\n"
      "s_nop 1\n"
      "v_mfma_f32_16x16x32_bf16 v[152:155], v[148:151], v[80:83], v[152:155]\n"
      "s_waitcnt vmcnt(6)\n"
      "global_load_dwordx4 v[112:115], v182, s[40:41] nt\n"
      "v_and_b32_dpp v148, v160, v144 row_newbcast:5 row_mask:0xf bank_mask:0xf\n"
      "v_and_b32_dpp v149, v160, v145 row_newbcast:5 row_mask:0xf bank_mask:0xf\n"
      "v_and_b32_dpp v150, v160, v146 row_newbcast:5 row_mask:0xf bank_mask:0xf\n"
      "v_and_b32_dpp v151, v160, v147 row_newbcast:5 row_mask:0xf bank_mask:0xf\n"
      "s_nop 1\n"
      "v_mfma_f32_16x16x32_bf16 v[152:155], v[148:151], v[84:87], v[152:155]\n"
      "s_waitcnt vmcnt(6)\n"
      "global_load_dwordx4 v[116:119], v183, s[40:41] nt\n"
      "v_and_b32_dpp v148, v160, v144 row_newbcast:9 row_mask:0xf bank_mask:0xf\n"
      "v_and_b32_dpp v149, v160, v145 row_newbcast:9 row_mask:0xf bank_mask:0xf\n"
      "v_and_b32_dpp v150, v160, v146 row_newbcast:9 row_mask:0xf bank_mask:0xf\n"
      "v_and_b32_dpp v151, v160, v147 row_newbcast:9 row_mask:0xf bank_mask:0xf\n"
      "s_nop 1\n"
      "v_mfma_f32_16x16x32_bf16 v[152:155], v[148:151], v[88:91], v[152:155]\n"
      "s_waitcnt vmcnt(6)\n"
      "global_load_dwordx4 v[120:123], v184, s[40:41] nt\n"
      "v_and_b32_dpp v148, v160, v144 row_newbcast:13 row_mask:0xf bank_mask:0xf\n"
      "v_and_b32_dpp v149, v160, v145 row_newbcast:13 row_mask:0xf bank_mask:0xf\n"
      "v_and_b32_dpp v150, v160, v146 row_newbcast:13 row_mask:0xf bank_mask:0xf\n"
      "v_and_b32_dpp v151, v160, v147 row_newbcast:13 row_mask:0xf bank_mask:0xf\n"
      "s_nop 1\n"
      "v_mfma_f32_16x16x32_bf16 v[152:155], v[148:151], v[92:95], v[152:155]\n"
      "s_waitcnt vmcnt(6)\n"
      "global_load_dwordx4 v[124:127], v185, s[40:41] nt\n"
      "v_and_b32_dpp v148, v160, v144 row_newbcast:2 row_mask:0xf bank_mask:0xf\n"
      "v_and_b32_dpp v149, v160, v145 row_newbcast:2 row_mask:0xf bank_mask:0xf\n"
      "v_and_b32_dpp v150, v160, v146 row_newbcast:2 row_mask:0xf bank_mask:0xf\n"
      "v_and_b32_dpp v151, v160, v147 row_newbcast:2 row_mask:0xf bank_mask:0xf\n"
      "s_nop 1\n"
      "v_mfma_f32_16x16x32_bf16 v[152:155], v[148:151], v[96:99], v[152:155]\n"
      "s_waitcnt vmcnt(6)\n"
      "global_load_dwordx4 v[64:67], v170, s[42:43] nt\n"
      "v_and_b32_dpp v148, v160, v144 row_newbcast:6 row_mask:0xf bank_mask:0xf\n"
      "v_and_b32_dpp v149, v160, v145 row_newbcast:6 row_mask:0xf bank_mask:0xf\n"
      "v_and_b32_dpp v150, v160, v146 row_newbcast:6 row_mask:0xf bank_mask:0xf\n"
      "v_and_b32_dpp v151, v160, v147 row_newbcast:6 row_mask:0xf bank_mask:0xf\n"
      "s_nop 1\n"
      "v_mfma_f32_16x16x32_bf16 v[152:155], v[148:151], v[100:103], v[152:155]\n"
      "s_waitcnt vmcnt(6)\n"
      "global_load_dwordx4 v[68:71], v171, s[42:43] nt\n"
      "v_and_b32_dpp v148, v160, v144 row_newbcast:10 row_mask:0xf bank_mask:0xf\n"
      "v_and_b32_dpp v149, v160, v145 row_newbcast:10 row_mask:0xf bank_mask:0xf\n"
      "v_and_b32_dpp v150, v160, v146 row_newbcast:10 row_mask:0xf bank_mask:0xf\n"
      "v_and_b32_dpp v151, v160, v147 row_newbcast:10 row_mask:0xf bank_mask:0xf\n"
      "s_nop 1\n"
      "v_mfma_f32_16x16x32_bf16 v[152:155], v[148:151], v[104:107], v[152:155]\n"
      "s_waitcnt vmcnt(6)\n"
      "global_load_dwordx4 v[72:75], v172, s[42:43] nt\n"
      "v_and_b32_dpp v148, v160, v144 row_newbcast:14 row_mask:0xf bank_mask:0xf\n"
      "v_and_b32_dpp v149, v160, v145 row_newbcast:14 row_mask:0xf bank_mask:0xf\n"
      "v_and_b32_dpp v150, v160, v146 row_newbcast:14 row_mask:0xf bank_mask:0xf\n"
      "v_and_b32_dpp v151, v160, v147 row_newbcast:14 row_mask:0xf bank_mask:0xf\n"
      "s_nop 1\n"
      "v_mfma_f32_16x16x32_bf16 v[152:155], v[148:151], v[108:111], v[152:155]\n"
      "s_waitcnt vmcnt(6)\n"
      "global_load_dwordx4 v[76:79], v173, s[42:43] nt\n"
      "v_and_b32_dpp v148, v160, v144 row_newbcast:3 row_mask:0xf bank_mask:0xf\n"
      "v_and_b32_dpp v149, v160, v145 row_newbcast:3 row_mask:0xf bank_mask:0xf\n"
      "v_and_b32_dpp v150, v160, v146 row_newbcast:3 row_mask:0xf bank_mask:0xf\n"
      "v_and_b32_dpp v151, v160, v147 row_newbcast:3 row_mask:0xf bank_mask:0xf\n"
      "s_nop 1\n"
      "v_mfma_f32_16x16x32_bf16 v[152:155], v[148:151], v[112:115], v[152:155]\n"
      "s_waitcnt vmcnt(6)\n"
      "global_load_dwordx4 v[80:83], v174, s[42:43] nt\n"
      "v_and_b32_dpp v148, v160, v144 row_newbcast:7 row_mask:0xf bank_mask:0xf\n"
      "v_and_b32_dpp v149, v160, v145 row_newbcast:7 row_mask:0xf bank_mask:0xf\n"
      "v_and_b32_dpp v150, v160, v146 row_newbcast:7 row_mask:0xf bank_mask:0xf\n"
      "v_and_b32_dpp v151, v160, v147 row_newbcast:7 row_mask:0xf bank_mask:0xf\n"
      "s_nop 1\n"
      "v_mfma_f32_16x16x32_bf16 v[152:155], v[148:151], v[116:119], v[152:155]\n"
      "s_waitcnt vmcnt(6)\n"
      "global_load_dwordx4 v[84:87], v175, s[42:43] nt\n"
      "v_and_b32_dpp v148, v160, v144 row_newbcast:11 row_mask:0xf bank_mask:0xf\n"
      "v_and_b32_dpp v149, v160, v145 row_newbcast:11 row_mask:0xf bank_mask:0xf\n"
      "v_and_b32_dpp v150, v160, v146 row_newbcast:11 row_mask:0xf bank_mask:0xf\n"
      "v_and_b32_dpp v151, v160, v147 row_newbcast:11 row_mask:0xf bank_mask:0xf\n"
      "s_nop 1\n"
      "v_mfma_f32_16x16x32_bf16 v[152:155], v[148:151], v[120:123], v[152:155]\n"
      "ds_read_b32 v159, v169\n"
      "ds_read_b32 v168, v165 offset:32\n"
      "ds_read_b32 v157, v165 offset:48\n"
      "ds_read_b32 v158, v164\n"
      "s_waitcnt vmcnt(6)\n"
      "global_load_dwordx4 v[88:91], v176, s[42:43] nt\n"
      "v_and_b32_dpp v148, v160, v144 row_newbcast:15 row_mask:0xf bank_mask:0xf\n"
      "v_and_b32_dpp v149, v160, v145 row_newbcast:15 row_mask:0xf bank_mask:0xf\n"
      "v_and_b32_dpp v150, v160, v146 row_newbcast:15 row_mask:0xf bank_mask:0xf\n"
      "v_and_b32_dpp v151, v160, v147 row_newbcast:15 row_mask:0xf bank_mask:0xf\n"
      "s_nop 1\n"
      "v_mfma_f32_16x16x32_bf16 v[152:155], v[148:151], v[124:127], v[152:155]\n"
      "s_mov_b32 s59, 1\n"
      "s_branch LT_%=\n"
      "LE_%=:\n"
      :
      : [lane] "v"(lane), [wv] "s"(wv), [rot] "s"(rot), [kb] "s"(kb), [vb] "s"(vb), [rb] "s"(rb), [r0] "s"(r0),
        [lq] "s"(lds0 + L_Q), [lso] "s"(lds0 + L_SOWN + wv * 4096), [lpo] "s"(lds0 + L_PROG + wv * 4),
        [lout] "s"(lds0 + L_OUT + wv * 512),
        [tp0] "s"(w1 + (size_t)80 * m * FF + 64 * min(h, NG - 1)), [tp1] "s"(w3 + (size_t)80 * m * FF + 64 * min(h, NG - 1)),
        [tp2] "s"(m < 5 ? wo + (size_t)h * DH * DM : w2 + (size_t)64 * min(h, NG - 1) * DM)
      : "v64", "v65", "v66", "v67", "v68", "v69", "v70", "v71", "v72", "v73", "v74", "v75", "v76", "v77", "v78", "v79", "v80", "v81", "v82", "v83", "v84", "v85", "v86", "v87", "v88", "v89", "v90", "v91", "v92", "v93", "v94", "v95", "v96", "v97", "v98", "v99", "v100", "v101", "v102", "v103", "v104", "v105", "v106", "v107", "v108", "v109", "v110", "v111", "v112", "v113", "v114", "v115", "v116", "v117", "v118", "v119", "v120", "v121", "v122", "v123", "v124", "v125", "v126", "v127", "v128", "v129", "v130", "v131", "v132", "v133", "v134", "v135", "v136", "v137", "v138", "v139", "v140", "v141", "v142", "v143", "v144", "v145", "v146", "v147", "v148", "v149", "v150", "v151", "v152", "v153", "v154", "v155", "v156", "v157", "v158", "v159", "v160", "v161", "v162", "v163", "v164", "v165", "v166", "v167", "v168", "v169", "v170", "v171", "v172", "v173", "v174", "v175", "v176", "v177", "v178", "v179", "v180", "v181", "v182", "v183", "v184", "v185", "s40", "s41", "s42", "s43", "s44", "s45", "s46", "s47", "s48", "s49", "s50", "s51", "s52", "s53", "s54", "s55", "s56", "s57", "s58", "s59", "s60", "s61", "s62", "s63", "s64", "s65", "s66", "s67", "s68", "s69", "s70", "s71", "s72", "s73", "s74", "s75", "s76", "s77", "s78", "vcc", "scc", "memory");
    return;
  }

  const bool do_g = h < NG, do_w2 = do_g && m >= 5, do_wo = m < 5;
  const int red = (j != 0) ? -1 : (m == 7) ? h : (m == 6 && h >= 24) ? 8 + h : -1;
  const unsigned lp = lds0 + L_PART + j * 512 + 32 * c + (lane & 16), lq = lds0 + L_PART + 256 * j + 4 * lane;

  unsigned ep = __hip_atomic_load((const unsigned*)(ws + WS_EPOCH), RLX_AGENT);
  asm volatile("" ::: "memory");
  const u32x4 xa = *(const u32x4_a4*)((const char*)x + ox);
  const unsigned xb = *(const unsigned*)((const char*)x + ox + 16);
  const unsigned xy = *(const unsigned*)((const char*)x + oy);
  u32x4 wa[5], wg[5], wr[8];
#pragma unroll
  for (int i = 0; i < 5; ++i) wa[i] = ld16((const u16*)((const char*)wq + i * (4 * NH * DH * 2) + oq));
  u32x4 msk;
#pragma unroll
  for (int k = 0; k < 4; ++k) msk[k] = (c >> 1) == k ? 0xffffu << (16 * (c & 1)) : 0u;
  asm volatile("" : "+v"(ep) :: "memory");
  const unsigned tag = __builtin_amdgcn_readfirstlane(ep) + 1u;
  if (j == 2) {
    unsigned lv = 0;
    for (unsigned it = 0; (lv = LDS(unsigned, lds0 + L_CNT + 8)) == 0u && it < SPIN_L; ++it) {}
    if (lane == 0) put(ws + WS_VOTE + b, tag, lv);
  }
  {
    float xv[10];
#pragma unroll
    for (int k = 0; k < 4; ++k) { xv[2 * k] = bflo(xa[k]); xv[2 * k + 1] = bfhi(xa[k]); }
    xv[8] = bflo(xb); xv[9] = bfhi(xb);
    float s = 0.f;
#pragma unroll
    for (int k = 0; k < 10; ++k) s += xv[k];
    const float mean = wave_sum(s) * (1.f / DM);
    float v = 0.f;
#pragma unroll
    for (int k = 0; k < 10; ++k) { const float d = xv[k] - mean; v = fmaf(d, d, v); }
    const float var = wave_sum(v) * (1.f / DM);
    const float mb = rbf(mean), den = rbf(__builtin_amdgcn_sqrtf(rbf(rbf(var) + 1e-5f)));
    const float rd = __builtin_amdgcn_rcpf(den);
    const float d0 = rbf(bflo(xy) - mb), d1 = rbf(bfhi(xy) - mb), q0 = d0 * rd, q1 = d1 * rd;
    if (lane < 40)
      LDS(u64, lds0 + L_XN + j * 320 + lane * 8) =
          ((u64)pair_bf(fmaf(fmaf(-q1, den, d1), rd, q1)) << 32) | pair_bf(fmaf(fmaf(-q0, den, d0), rd, q0));
  }
  {
    f32x4 a = {0.f, 0.f, 0.f, 0.f};
#pragma unroll
    for (int i = 0; i < 5; ++i) a = mac4(a, LDS(unsigned, lds0 + L_XN + j * 320 + (20 * j + 4 * i + r4) * 4), wa[i], msk);
    if (lane < 32) LDS(f32x4, lp) = a;
  }
  if (lane == 0) __hip_atomic_fetch_add((LAS unsigned*)(lds0 + L_CNT), 1u, __ATOMIC_RELAXED, __HIP_MEMORY_SCOPE_WORKGROUP);
  if (j < 2) {
    for (unsigned it = 0; LDS(unsigned, lds0 + L_CNT) < 4u && it < SPIN_L; ++it) {}
    put(ws + WS_QP + (size_t)b * 128 + 64 * j + lane, tag, sum4(lq));
  }
  if (j == 3) {
    const u64* vp = ws + WS_VOTE + lane;
    const u64* const g[4] = {vp, vp + 64, vp + 128, vp + 192};
    u64 r1[4], r2[4];
    float v[4];
    fetch(g, r1);
    __builtin_amdgcn_s_sleep(10);
    fetch(g, r2);
    if (!check(r1, tag, v))
      if (!check(r2, tag, v)) gather<4, 0>(g, tag, v);
    float t[4], mu = 4096.f;
#pragma unroll
    for (int k = 0; k < 4; ++k) { t[k] = (float)(__float_as_uint(v[k]) & 0xfffu); mu = fminf(mu, t[k] < 400.f ? 4096.f : t[k]); }
    mu = fminf(mu, dpp<ROR8>(mu));
    mu = -max32(-mu);
    float sm = 0.f;
#pragma unroll
    for (int k = 0; k < 4; ++k) {
      t[k] = t[k] < 400.f ? 260.f : fmaf(t[k] - mu, 0.25f, fminf(t[k] - mu, 420.f));
      sm += (((__float_as_uint(v[k]) >> 18) ^ (lane >> 3)) & 1) ? t[k] : -t[k];
    }
    sm = row_sum(sm);
    sm = add32(sm, sm);
    const unsigned neg = __float_as_uint(sm) >> 31, u2 = (lane ^ (lane >> 5)) & 1;
    float sq = 0.f;
#pragma unroll
    for (int k = 0; k < 4; ++k) {
      const unsigned u = __float_as_uint(v[k]);
      const bool far = ((u >> 18) ^ (lane >> 3) ^ neg) & 1;
      sq += ((far ? u2 ^ 1u : u2 ^ (u >> 16)) & 1) ? t[k] : -t[k];
    }
    sq = row_sum(sq);
    sq = add32(sq, sq);
    const unsigned sK = lane_of(sm, 0) >> 31, sV = lane_of(sm, 16) >> 31;
    const unsigned qK = lane_of(sq, 0) >> 31, qV = lane_of(sq, 16) >> 31;
    const unsigned e0 = (lane_of(v[0], 0) >> 16) & 1u;
    const unsigned ex = sK ^ sV, fK = qK ^ e0, fV = ex ? qV ^ e0 : fK, pp = pc ^ fK ^ fV;
    const unsigned sd = (lane_of(v[0], pc) >> 18) & 1u;
    const unsigned sp = (lane_of(v[0], pp) >> 18) & 1u;
    const unsigned dK = sK ^ sd, dV = sV ^ sd, mp = pp | ((side ^ dV ^ sK ^ sp) << 2);
    if (lane == 0) LDS(unsigned, lds0 + L_CNT + 4) = (dK << 1) | (fK << 2) | (dV << 5) | (fV << 6) | (ex << 8) | (mp << 12) | 0x8000u;
  } else {
    __builtin_amdgcn_s_sleep(24);
  }
  {
    const u64* base = ws + WS_QP + (size_t)(h * 8 + 4 * (lane >> 5)) * 128 + 32 * j + (lane & 31);
    const u64* const g[4] = {base, base + 128, base + 256, base + 384};
    u64 raw[4];
    float v[4];
    fetch(g, raw);
    {
      const u16* wp = do_g ? ((j & 2) ? w3 : w1) + 64 * h + 8 * (lane & 7) + (size_t)(80 * m + 40 * (j & 1) + (lane >> 3)) * FF : x;
      const int sg = do_g ? 8 * FF : 0;
#pragma unroll
      for (int i = 0; i < 5; ++i) wg[i] = ld16(wp + i * sg);
      const u16* p0 = do_wo ? wo + (size_t)(h * DH + 32 * j + r4) * DM + 128 * m + 8 * c
                    : do_w2 ? w2 + (size_t)(64 * h + 16 * j + r4) * DM + 256 * (m - 5) + 8 * c : x;
      const u16* p1 = do_wo ? p0 + 16 * DM : (do_w2 && m < 7) ? p0 + 128 : x;
      const int s0 = (do_wo || do_w2) ? 4 * DM : 0, s1 = (do_wo || (do_w2 && m < 7)) ? 4 * DM : 0;
#pragma unroll
      for (int i = 0; i < 4; ++i) wr[i] = ld16(p0 + i * s0);
#pragma unroll
      for (int i = 0; i < 4; ++i) wr[4 + i] = ld16(p1 + i * s1);
      asm volatile("" ::: "memory");
    }
    const bool ok = check(raw, tag, v);
    if (!ok) gather<4, 0>(g, tag, v);
    const float s = ((v[0] + v[1]) + v[2]) + v[3];
    const float q = add32(s, s);
    if (lane < 32) LDS(u16, lds0 + L_Q + (32 * j + lane) * 2) = f2bf(q);
  }
  asm volatile("s_waitcnt lgkmcnt(0)\n\ts_barrier" ::: "memory");
  const unsigned dec = __builtin_amdgcn_readfirstlane(LDS(unsigned, lds0 + L_CNT + 4));
  const bool exch = dec & 256;
  const unsigned bv = (b & ~7) | (m ^ ((dec >> 6) & 1u) ^ ((dec >> 3) & 4u));

  if (do_g) {
    float g[8] = {0.f, 0.f, 0.f, 0.f, 0.f, 0.f, 0.f, 0.f};
#pragma unroll
    for (int i = 0; i < 5; ++i) {
      const float s = bfhi(LDS(unsigned, lds0 + L_XN + j * 320 + (40 * (j & 1) + 8 * i + (lane >> 3)) * 4));
#pragma unroll
      for (int k = 0; k < 4; ++k) { g[2 * k] = fmaf(s, bflo(wg[i][k]), g[2 * k]); g[2 * k + 1] = fmaf(s, bfhi(wg[i][k]), g[2 * k + 1]); }
    }
    const float r0 = add16(add32(g[0], g[4]), add32(g[2], g[6])), r1 = add16(add32(g[1], g[5]), add32(g[3], g[7]));
    const bool hi = lane & 8;
    const int cg = 8 * (lane & 7) + 4 * (r4 >> 1) + 2 * (r4 & 1) + (hi ? 1 : 0);
    put(ws + WS_GP + ((size_t)b * 2 + (j & 1)) * 128 + 64 * (j >> 1) + cg, tag, (hi ? r1 : r0) + dpp<ROR8>(hi ? r0 : r1));
  }
  if (do_w2) {
    const u64* base = ws + WS_GP + (size_t)(h * 16 + 4 * (c >> 2)) * 128 + 16 * j + 4 * (c & 3) + r4;
    const u64* const g[8] = {base, base + 128, base + 256, base + 384, base + 64, base + 192, base + 320, base + 448};
    float v[8];
    __builtin_amdgcn_s_sleep(64);
    gather<8, 32>(g, tag, v);
    float gt = (v[0] + v[1]) + (v[2] + v[3]), up = (v[4] + v[5]) + (v[6] + v[7]);
    gt += dpp<ROR8>(gt); gt = rbf(gt + dpp<ROR4>(gt));
    up += dpp<ROR8>(up); up = rbf(up + dpp<ROR4>(up));
    const unsigned hv = pair_bf(rbf(gt / (1.f + __expf(-gt))) * up);
    const unsigned h0 = dppu<BCAST>(hv), h1 = dppu<BCAST + 1>(hv), h2 = dppu<BCAST + 2>(hv), h3 = dppu<BCAST + 3>(hv);
#pragma unroll
    for (int s = 0; s < 2; ++s) {
      f32x4 a = {0.f, 0.f, 0.f, 0.f};
      a = mac4(a, h0, wr[4 * s], msk); a = mac4(a, h1, wr[4 * s + 1], msk); a = mac4(a, h2, wr[4 * s + 2], msk); a = mac4(a, h3, wr[4 * s + 3], msk);
      if (lane < 32) LDS(f32x4, lp + s * 2048) = a;
    }
    if (lane == 0) __hip_atomic_fetch_add((LAS unsigned*)(lds0 + L_CNT), 1u, __ATOMIC_RELAXED, __HIP_MEMORY_SCOPE_WORKGROUP);
    if (j < 2 || m < 7) {
      for (unsigned it = 0; LDS(unsigned, lds0 + L_CNT) < 8u && it < SPIN_L; ++it) __builtin_amdgcn_s_sleep(1);
      put(ws + WS_BP + (size_t)h * DM + 256 * (m - 5) + 64 * j + lane, tag, sum4(lds0 + L_PART + (j >> 1) * 2048 + 256 * (j & 1) + 4 * lane));
    }
  }
  if (j == 1 && exch) {
    const unsigned bp = (b & ~7) | ((dec >> 12) & 7u);
#pragma unroll 1
    for (int kc = 0; kc < 2; ++kc) {
      wait_min4<8>(lds0 + L_PROG, 8 * (kc + 1));
      const unsigned no = (kc * 256 + b) * 2, ni = (kc * 256 + bp) * 2, lo = (r4 << 15) + c * 16;
      char* po = (char*)(ws + WS_SX) + ((no >> 7) << 18) + lo;
      const char* pi = (const char*)(ws + WS_SX) + ((ni >> 7) << 18) + lo;
#define XO(n, q) (((((n) & 127) + ((q) >> 1)) ^ r4 ^ (((q) & 1) << 2)) * 256 + (((q) & 1) << 17))
#pragma unroll
      for (int q = 0; q < 4; ++q) {
        const volatile LAS unsigned* s = (const volatile LAS unsigned*)(lds0 + L_SOWN + q * 4096 + kc * 2048 + lane * 4);
        const u32x4 d = {(s[0] >> 16) | s[64], (s[128] >> 16) | s[192], (s[256] >> 16) | s[320], (s[384] >> 16) | s[448]};
        asm volatile("global_store_dwordx4 %0, %1, off sc1 nt\n\ts_nop 1" :: "v"(po + XO(no, q)), "v"(d) : "memory");
      }
      asm volatile("s_waitcnt vmcnt(0)" ::: "memory");
      if (lane == 0) put(ws + WS_XF + b, tag, (unsigned)kc + 1u);
      for (unsigned it = 0; it < SPIN_G; ++it) {
        __builtin_amdgcn_s_sleep(64);
        const u64 f = __hip_atomic_load(ws + WS_XF + bp, RLX_AGENT);
        if ((unsigned)(f >> 32) == tag && (unsigned)f > (unsigned)kc) break;
      }
      u32x4 g[4];
      asm volatile("global_load_dwordx4 %0, %4, off sc1 nt\n\tglobal_load_dwordx4 %1, %5, off sc1 nt\n\t"
                   "global_load_dwordx4 %2, %6, off sc1 nt\n\tglobal_load_dwordx4 %3, %7, off sc1 nt\n\t"
                   "s_waitcnt vmcnt(0)" : "=&v"(g[0]), "=&v"(g[1]), "=&v"(g[2]), "=&v"(g[3]) : "v"(pi + XO(ni, 0)), "v"(pi + XO(ni, 1)), "v"(pi + XO(ni, 2)), "v"(pi + XO(ni, 3)) : "memory");
#pragma unroll
      for (int q = 0; q < 4; ++q) {
        volatile LAS unsigned* d = (volatile LAS unsigned*)(lds0 + L_SIN + q * 4096 + kc * 2048 + lane * 4);
#pragma unroll
        for (int k = 0; k < 4; ++k) { if (k || kc || (c & 3) >= NS) d[128 * k] = g[q][k] << 16; d[128 * k + 64] = g[q][k] & 0xffff0000u; }
      }
      for (unsigned it = 0; LDS(unsigned, lds0 + L_CNT + 16) == 0u && it < SPIN_L; ++it) __builtin_amdgcn_s_sleep(8);
      if (lane < 4) LDS(unsigned, lds0 + L_PROG + 16 + lane * 4) = 8u * (kc + 1);
    }
  }
  if (j == 2) {
    const unsigned ls = lds0 + L_SOWN + lane * 4, ld = ls + (exch ? L_SIN - L_SOWN : 0);
#pragma unroll 1
    for (int k = 0; k < 3; ++k) {
      const unsigned n = k == 2 ? CP2 : k ? CP1 : CP0;
      float mw = -INFINITY, lw = 0.f;
      if (k == 2) {
        wait_min4<8>(lds0 + L_PROG, 16);
        wait_min4<8>(lds0 + L_PROG + 16, 16);
        const volatile LAS f32x4* sp = (const volatile LAS f32x4*)(lds0 + (exch ? L_SIN : L_SOWN)) + lane;
#pragma unroll 4
        for (int i = 0; i < 16; ++i) { const f32x4 s = sp[64 * i]; mw = fmaxf(fmaxf(mw, fmaxf(s[0], s[1])), fmaxf(s[2], s[3])); }
        mw = fmaxf(mw, dpp<ROR8>(mw)); mw = fmaxf(mw, dpp<ROR4>(mw)); mw = fmaxf(mw, dpp<ROR2>(mw)); mw = fmaxf(mw, dpp<ROR1>(mw));
        mw = max32(max16(mw));
#pragma unroll 4
        for (int i = 0; i < 16; ++i) {
          const f32x4 s = sp[64 * i];
#pragma unroll
          for (int u = 0; u < 4; ++u) lw += __builtin_amdgcn_exp2f((s[u] - mw) * LOG2E);
        }
        lw = wave_sum(lw);
      }
      wait_min4<2>(lds0 + L_PROG, n);
      const unsigned t = (unsigned)__builtin_amdgcn_s_memrealtime();
      if (lane == 0) put(ws + WS_PG + (k * 8 + m) * 32 + h, tag, t);
      if (k == 2) {
        if (lane < 2) put(ws + WS_ML + bv * 2 + lane, tag, lane ? lw : mw);
      } else if (k == 0) {
        put(ws + WS_X0 + b * 128 + lane, tag, (LDS(unsigned, ls) >> 16) | LDS(unsigned, ls + 4096));
        put(ws + WS_X0 + b * 128 + 64 + lane, tag, (LDS(unsigned, ls + 8192) >> 16) | LDS(unsigned, ls + 12288));
      }
      wait_min4<8>(lds0 + L_PROG, n + 2);
      u64 gg[4];
#pragma unroll
      for (int u = 0; u < 4; ++u) gg[u] = __hip_atomic_load(ws + WS_PG + k * 256 + u * 64 + lane, RLX_AGENT);
      if (k == 2) {
        const u64* const gm[2] = {ws + WS_ML + (h * 8 + (lane & 7)) * 2, ws + WS_ML + (h * 8 + (lane & 7)) * 2 + 1};
        float vm[2];
        gather<2, 32>(gm, tag, vm);
        float M = fmaxf(vm[0], dpp<ROR4>(vm[0]));
        M = fmaxf(M, dpp<ROR2>(M)); M = fmaxf(M, dpp<ROR1>(M));
        float L = vm[1] * __builtin_amdgcn_exp2f((vm[0] - M) * LOG2E);
        L += dpp<ROR4>(L); L += dpp<ROR2>(L); L += dpp<ROR1>(L);
        const unsigned z = __builtin_amdgcn_readfirstlane(__float_as_uint(fmaf(__builtin_amdgcn_logf(L), LN2, M)));
        if (lane < 4) LDS(unsigned, lds0 + L_PROG + 48 + lane * 4) = z;
      } else if (k == 0) {
        const unsigned b0 = (b & ~7) | (pc ^ ((dec >> 6) & 1u)) | ((side ^ ((dec >> 5) & 1u)) << 2);
        const u64* const gx[2] = {ws + WS_X0 + b0 * 128 + lane, ws + WS_X0 + (b0 ^ 4) * 128 + 64 + lane};
        float vx[2];
        gather<2, 32>(gx, tag, vx);
#pragma unroll
        for (int u = 0; u < 2; ++u) if ((c & 3) < NS) {
          LDS(unsigned, ld + u * 8192) = __float_as_uint(vx[u]) << 16;
          LDS(unsigned, ld + u * 8192 + 4096) = __float_as_uint(vx[u]) & 0xffff0000u;
        }
        if (exch) LDS(unsigned, lds0 + L_CNT + 16) = 1u;
        else if (lane < 4) LDS(unsigned, lds0 + L_PROG + 16 + lane * 4) = 16u;
      }
      float dt = 0.f, cn = 0.f;
#pragma unroll
      for (int u = 0; u < 4; ++u) {
        const float w = (unsigned)(gg[u] >> 32) != tag ? 0.f : (u * 2 + (lane >> 5)) == m ? 7.f : 1.f;
        dt += w * (float)(int)(t - (unsigned)gg[u]); cn += w;
      }
      dt = wave_sum(dt); cn = wave_sum(cn);
      const int lag = __builtin_amdgcn_readfirstlane((int)(dt * __builtin_amdgcn_rcpf(fmaxf(cn, 1.f))));
      const int s0 = max((int)lds_min4(lds0 + L_PROG) - 14, 1);
      if (lane < 4) LDS(int, lds0 + L_PROG + 32 + lane * 4) = lag > DEADB ? s0 + min((lag - DEADB + GAINB / 2) / GAINB, CP1 - CP0) : 0;
    }
  }
  float bsum = 0.f, xres = 0.f;
  if (red >= 0) {
    wait_min4<8>(lds0 + L_PROG, 4);
    const u64* base = ws + WS_BP + 16 * red + c;
    const u64* const g[7] = {base + (r4 + 0) * DM, base + (r4 + 4) * DM, base + (r4 + 8) * DM, base + (r4 + 12) * DM,
                             base + (r4 + 16) * DM, base + (r4 + 20) * DM, base + (r4 == 3 ? 0 : r4 + 24) * DM};
    float v[7];
    gather<7, 16>(g, tag, v);
    float s = ((v[0] + v[1]) + (v[2] + v[3])) + ((v[4] + v[5]) + (r4 == 3 ? 0.f : v[6]));
    s = add16(s, s);
    bsum = rbf(add32(s, s));
    xres = bflo(x[16 * red + c]);
  }
  typedef __attribute__((address_space(1))) u64* gp64;
  gp64 pp_o = (gp64)(ws + WS_PP + (size_t)bv * 128 + 64 * (j & 1) + lane);
  gp64 pp_i = (gp64)(red >= 0 ? ws + WS_AP + (size_t)red * 512 + lane
                              : ws + WS_PP + (size_t)(h * 8 + 4 * (c >> 3)) * 128 + 32 * j + 4 * (c & 7) + r4);
  gp64 ap_o = (gp64)(ws + WS_AP + (size_t)(8 * m + 4 * (j & 1) + r4) * 512 + h * 16 + c);
  unsigned lo_i = lds0 + L_OUT + 256 * (j & 1) + 4 * lane;
  __builtin_amdgcn_s_setprio(3);
  asm volatile("s_waitcnt vmcnt(0) lgkmcnt(0)\n\ts_barrier"
               : "+v"(pp_o), "+v"(pp_i), "+v"(ap_o), "+v"(lo_i), "+v"(bsum), "+v"(xres), "+v"(wr[0]), "+v"(wr[1]), "+v"(wr[2]),
                 "+v"(wr[3]), "+v"(wr[4]), "+v"(wr[5]), "+v"(wr[6]), "+v"(wr[7]) :: "memory");
  if (j < 2) put((u64*)pp_o, tag, sum4(lo_i));
  if (do_wo) {
    float v[4];
    poll4((const u64*)pp_i, tag, v);
    const float o = (v[0] + v[1]) + (v[2] + v[3]);
    const unsigned on = pair_bf(o + dpp<ROR8>(o));
    f32x4 a = {0.f, 0.f, 0.f, 0.f}, a2 = a;
    a = mac4(a, dppu<BCAST>(on), wr[0], msk);     a2 = mac4(a2, dppu<BCAST + 1>(on), wr[1], msk);
    a = mac4(a, dppu<BCAST + 2>(on), wr[2], msk); a2 = mac4(a2, dppu<BCAST + 3>(on), wr[3], msk);
    a = mac4(a, dppu<BCAST + 4>(on), wr[4], msk); a2 = mac4(a2, dppu<BCAST + 5>(on), wr[5], msk);
    a = mac4(a, dppu<BCAST + 6>(on), wr[6], msk); a2 = mac4(a2, dppu<BCAST + 7>(on), wr[7], msk);
    if (lane < 32) LDS(f32x4, lp) = a + a2;
    asm volatile("s_waitcnt lgkmcnt(0)\n\ts_barrier" ::: "memory");
    if (j < 2) put((u64*)ap_o, tag, sum4(lq));
  }
  if (red >= 0) {
    float v[8];
    poll8((const u64*)pp_i, tag, v);
    float s = ((v[0] + v[1]) + (v[2] + v[3])) + ((v[4] + v[5]) + (v[6] + v[7]));
    s = add16(s, s);
    const float A = rbf(add32(s, s));
    if (lane < 16) out[16 * red + lane] = f2bf(rbf(xres + A) + bsum);
    if (red == 0 && lane == 0) __hip_atomic_store((unsigned*)(ws + WS_EPOCH), tag, RLX_AGENT);
  }
}

extern "C" void kernel_launch(void* const* d_in, const int* in_sizes, int n_in, void* d_out, int out_size, void* d_ws,
                              size_t ws_size, hipStream_t stream) {
  fused_decode<<<256, 512, 0, stream>>>((const u16*)d_in[0], (const char*)d_in[1], (const char*)d_in[2], (const u16*)d_in[3],
                                        (const u16*)d_in[4], (const u16*)d_in[6], (const u16*)d_in[7], (const u16*)d_in[8],
                                        (u16*)d_out, (u64*)d_ws);
}
```
